```python
import torch

def _softmax(x, axis=-1):
    return torch.softmax(x, dim=axis)

def _gelu(x):
    return torch.nn.functional.gelu(x, approximate='tanh')

def _silu(x):
    return torch.nn.functional.silu(x)

def _sigmoid(x):
    return torch.sigmoid(x)

def _layernorm(x, axis=-1, eps=1e-05):
    m = x.mean(dim=axis, keepdim=True)
    v = x.var(dim=axis, keepdim=True, unbiased=False)
    return (x - m) / torch.sqrt(v + eps)

def _scatter_add(tgt, idx, src, axis=0):
    return tgt.clone().index_add_(axis, idx, src)

def _segment_sum(src, seg, n):
    out = torch.zeros((n,) + src.shape[1:], dtype=src.dtype)
    return out.index_add_(0, seg, src)

def _pad(x, pw):
    flat = tuple((v for pair in reversed(pw) for v in pair))
    return torch.nn.functional.pad(x, flat)
kernel_name = 'adaLN_cond__grouped_kv_mqa__dense_activation_silu__prompt'

def setup_inputs():

    _w = lambda a: (lambda t: t.to(torch.bfloat16) if t.is_floating_point() else t)(torch.as_tensor(a))

    def _A_setup():
        B, S, D, H = (1, 2048, 512, 32)
        Dh = D // H
        return {'x': _w((lambda *s: torch.randn(*s, dtype=torch.bfloat16) * 0.05)(B, S, D)), 'wq': _w((lambda *s: torch.randn(*s, dtype=torch.bfloat16) * 0.05)(D, H * Dh)), 'wk': _w((lambda *s: torch.randn(*s, dtype=torch.bfloat16) * 0.05)(D, Dh)), 'wv': _w((lambda *s: torch.randn(*s, dtype=torch.bfloat16) * 0.05)(D, Dh)), 'wo': _w((lambda *s: torch.randn(*s, dtype=torch.bfloat16) * 0.05)(H * Dh, D))}
    _A_in = _A_setup()

    def _B_setup():
        B, S, D, F = (1, 2048, 512, 1792)
        return {'x': _w((lambda *s: torch.randn(*s, dtype=torch.bfloat16) * 0.05)(B, S, D)), 'w1': _w((lambda *s: torch.randn(*s, dtype=torch.bfloat16) * 6.4)(D, F) * D ** (-0.5)), 'w2': _w((lambda *s: torch.randn(*s, dtype=torch.bfloat16) * 0.2)(F, D) * F ** (-0.5))}
    _B_in = _B_setup()
    return {'A_x': _A_in['x'], 'A_wq': _A_in['wq'], 'A_wk': _A_in['wk'], 'A_wv': _A_in['wv'], 'A_wo': _A_in['wo'], 'B_x': _B_in['x'], 'B_w1': _B_in['w1'], 'B_w2': _B_in['w2'], 'c': _w((lambda *s: torch.randn(*s, dtype=torch.bfloat16) * 0.1)(1, 512) * 8.0), 'w_mod': _w((lambda *s: torch.randn(*s, dtype=torch.bfloat16) * 0.1)(512, 6 * 512) * 512 ** (-0.5) * 8.0)}

def reference(A_x, A_wq, A_wk, A_wv, A_wo, B_x, B_w1, B_w2, c, w_mod):

    def _A_ref(x, wq, wk, wv, wo):
        B, S, D = x.shape
        H = 32
        Dh = D // H
        q = (lambda x, *d: x.permute(*d))((x @ wq).reshape(B, S, H, Dh), 0, 2, 1, 3)
        k = x @ wk
        v = x @ wv
        scores = torch.einsum('bhqd,bkd->bhqk', q, k) * Dh ** (-0.5)
        p = _softmax(scores, axis=-1)
        o = torch.einsum('bhqk,bkd->bhqd', p, v)
        return (lambda x, *d: x.permute(*d))(o, 0, 2, 1, 3).reshape(B, S, H * Dh) @ wo

    def _B_ref(x, w1, w2):
        h = torch.einsum('bsd,df->bsf', x, w1)
        h = _silu(h)
        out = torch.einsum('bsf,fd->bsd', h, w2)
        return out
    x = A_x
    mod = (_silu(c) @ w_mod).reshape(c.shape[0], 6, 1, -1)
    sa, ca, ga, sb, cb, gb = (mod[:, 0].clone(), mod[:, 1].clone(), mod[:, 2].clone(), mod[:, 3].clone(), mod[:, 4].clone(), mod[:, 5].clone())
    h = _layernorm(x, axis=-1) * (1 + ca) + sa
    x = x + ga * _A_ref(h, A_wq, A_wk, A_wv, A_wo)
    h = _layernorm(x, axis=-1) * (1 + cb) + sb
    x = x + gb * _B_ref(h, B_w1, B_w2)
    return x
```

```cpp
#include <hip/hip_runtime.h>
#include <hip/hip_bf16.h>
#include <stdint.h>

typedef __bf16 bf16x8 __attribute__((ext_vector_type(8)));
typedef __bf16 bf16x2 __attribute__((ext_vector_type(2)));
typedef float f32x2 __attribute__((ext_vector_type(2)));
typedef float f32x4 __attribute__((ext_vector_type(4)));
typedef float f32x16 __attribute__((ext_vector_type(16)));
typedef unsigned u32x4 __attribute__((ext_vector_type(4)));
typedef unsigned u32x2 __attribute__((ext_vector_type(2)));
typedef unsigned short u16;
#define LAS __attribute__((address_space(3)))
#define GAS __attribute__((address_space(1)))
#define RLX_AGENT __ATOMIC_RELAXED, __HIP_MEMORY_SCOPE_AGENT
#define SBAR() __builtin_amdgcn_sched_barrier(0)
#define PIN(x) asm volatile("" : "+v"(x))
#define DRAIN() asm volatile("s_waitcnt vmcnt(0)" ::: "memory")

constexpr int S_ = 2048, D_ = 512, H_ = 32, F_ = 1792;
constexpr float QSCALE = 0.25f * 1.4426950408889634f;
constexpr float LN_EPS = 1e-5f;
constexpr int AUX_SC1 = 16;

constexpr int WS_MOD = 0;
constexpr int WS_KMAX = 12288;
constexpr int WS_FLAG = 16384;
constexpr int WS_KV = 131072;
constexpr int WS_QB = 262144;
constexpr int WS_OB = WS_QB + 2097152;
constexpr int WS_X1 = WS_OB + 2097152;
constexpr int WS_HB = WS_X1 + 2097152;
constexpr int WS_WQ = WS_HB + 7340032;
constexpr int WS_WK = WS_WQ + 512 * 1024;
constexpr int WS_WV = WS_WK + 16 * 1024;
constexpr int WS_WO = WS_WV + 16 * 1024;
constexpr int WS_W1 = WS_WO + 512 * 1024;
constexpr int WS_W2 = WS_W1 + 1792 * 1024;
constexpr int WS_END = WS_W2 + 1792 * 1024;
constexpr int NFRAG = 512 + 16 + 16 + 512 + 1792 + 1792;

__device__ __forceinline__ unsigned pack2(float a, float b) {
  f32x2 v = {a, b};
  return __builtin_bit_cast(unsigned, __builtin_convertvector(v, bf16x2));
}
__device__ __forceinline__ float bflo(unsigned u) { return __builtin_bit_cast(float, u << 16); }
__device__ __forceinline__ float bfhi(unsigned u) { return __builtin_bit_cast(float, u & 0xffff0000u); }
__device__ __forceinline__ float bfr(float a) { return bflo(pack2(a, 0.f) & 0xffffu); }
__device__ __forceinline__ f32x16 mfma32(u32x4 a, u32x4 b, f32x16 c) {
  return __builtin_amdgcn_mfma_f32_32x32x16_bf16(__builtin_bit_cast(bf16x8, a), __builtin_bit_cast(bf16x8, b), c, 0, 0, 0);
}
__device__ __forceinline__ f32x4 mfma16(u32x4 a, u32x4 b, f32x4 c) {
  return __builtin_amdgcn_mfma_f32_16x16x32_bf16(__builtin_bit_cast(bf16x8, a), __builtin_bit_cast(bf16x8, b), c, 0, 0, 0);
}
__device__ __forceinline__ float dot2bf(unsigned a, unsigned b, float c) {
  return __builtin_amdgcn_fdot2_f32_bf16(__builtin_bit_cast(bf16x2, a), __builtin_bit_cast(bf16x2, b), c, false);
}
__device__ __forceinline__ float wave_sum(float v) {
#pragma unroll
  for (int o = 32; o >= 1; o >>= 1) v += __shfl_xor(v, o);
  return v;
}
__device__ __forceinline__ float wave_max(float v) {
#pragma unroll
  for (int o = 32; o >= 1; o >>= 1) v = fmaxf(v, __shfl_xor(v, o));
  return v;
}
__device__ __forceinline__ void glds16(const void* g, unsigned lds_base) {
  unsigned sv;
  asm volatile("s_mov_b32 %0, m0\n\ts_mov_b32 m0, %2\n\ts_nop 0\n\tglobal_load_lds_dwordx4 %1, off\n\ts_mov_b32 m0, %0"
               : "=&s"(sv) : "v"(g), "s"(lds_base) : "memory");
}
__device__ __forceinline__ u32x4 ldg16(const unsigned char* p) { return *(const GAS u32x4*)p; }

__device__ __forceinline__ void block_map(int bid, int& rb, int& m) {
  const int x = bid & 7, j = bid >> 3;
  rb = x * 4 + (j & 3);
  m = j >> 2;
}

__device__ __forceinline__ void ln_mod_store(const u32x4 (&xr)[8], const float (&sc)[8], const float (&sh)[8], LAS unsigned char* hbase,
                                             int row0, int lane) {
#pragma unroll
  for (int i = 0; i < 8; ++i) {
    float v[8];
#pragma unroll
    for (int q = 0; q < 4; ++q) { v[2 * q] = bflo(xr[i][q]); v[2 * q + 1] = bfhi(xr[i][q]); }
    float s = ((v[0] + v[1]) + (v[2] + v[3])) + ((v[4] + v[5]) + (v[6] + v[7]));
    const float mu = wave_sum(s) * (1.f / D_);
    float ss = 0.f;
#pragma unroll
    for (int q = 0; q < 8; ++q) { v[q] -= mu; ss = fmaf(v[q], v[q], ss); }
    const float rstd = __builtin_amdgcn_rsqf(wave_sum(ss) * (1.f / D_) + LN_EPS);
    u32x4 o;
#pragma unroll
    for (int q = 0; q < 4; ++q)
      o[q] = pack2(fmaf(v[2 * q] * rstd, sc[2 * q], sh[2 * q]), fmaf(v[2 * q + 1] * rstd, sc[2 * q + 1], sh[2 * q + 1]));
    const int row = row0 + i;
    *(LAS u32x4*)(hbase + row * 1024 + ((lane ^ (row & 15)) << 4)) = o;
  }
}

__global__ void __launch_bounds__(512, 2) k0_prep(const u16* __restrict__ wq, const u16* __restrict__ wk, const u16* __restrict__ wv,
                                                  const u16* __restrict__ wo, const u16* __restrict__ w1, const u16* __restrict__ w2,
                                                  const u16* __restrict__ cvec, const u16* __restrict__ wmod, unsigned char* __restrict__ ws) {
  __shared__ float red[8][32];
  const int bid = blockIdx.x, tid = threadIdx.x, lane = tid & 63, w = __builtin_amdgcn_readfirstlane(tid >> 6);
  if (tid < 3) *(unsigned*)(ws + WS_FLAG + (tid * 256 + bid) * 128) = 0u;
  u32x4 wm[4];
  u16 cv[4];
  if (bid < 96) {
#pragma unroll
    for (int i = 0; i < 4; ++i) {
      const int row = (tid >> 2) + 128 * i;
      wm[i] = *(const u32x4*)(wmod + (size_t)row * 3072 + bid * 32 + (tid & 3) * 8);
      cv[i] = cvec[row];
    }
  }
  for (int fid = bid * 8 + w; fid < NFRAG; fid += 2048) {
    const u16* src; int ld, rbase, col; bool perm = false;
    const int g = lane >> 4, c = lane & 15, hi = lane >> 5, c32 = lane & 31;
    if (fid < 512) { src = wq; ld = 512; rbase = 32 * (fid & 15) + 8 * g; col = 16 * (fid >> 4) + c; }
    else if (fid < 528) { src = wk; ld = 16; rbase = 32 * (fid - 512) + 8 * g; col = c; }
    else if (fid < 544) { src = wv; ld = 16; rbase = 32 * (fid - 528) + 8 * g; col = c; }
    else if (fid < 1056) { const int id = fid - 544, m = id >> 6, ks = (id >> 2) & 15, t = id & 3;
      src = wo; ld = 512; perm = true; rbase = 16 * (2 * ks + (g >> 1)) + 4 * (g & 1); col = 64 * m + 32 * (t >> 1) + 8 * (c >> 2) + 4 * (t & 1) + (c & 3); }
    else if (fid < 2848) { const int id = fid - 1056; src = w1; ld = F_; rbase = 16 * (id & 31) + 8 * hi; col = 32 * (id >> 5) + c32; }
    else { const int id = fid - 2848, m = id / 224, rem = id - m * 224, ks = rem >> 1, ct = rem & 1;
      src = w2; ld = 512; perm = true; rbase = 16 * ks + 4 * hi; col = 64 * m + 32 * ct + 16 * ((c32 >> 2) & 1) + (c32 & 3) + 4 * (c32 >> 3); }
    const u16* p = src + (size_t)rbase * ld + col;
    const int up = perm ? 8 : 4;
    u16 e[8];
#pragma unroll
    for (int j = 0; j < 4; ++j) { e[j] = p[j * ld]; e[4 + j] = p[(up + j) * ld]; }
    u32x4 o;
#pragma unroll
    for (int q = 0; q < 4; ++q) o[q] = (unsigned)e[2 * q] | ((unsigned)e[2 * q + 1] << 16);
    *(u32x4*)(ws + WS_WQ + (size_t)fid * 1024 + lane * 16) = o;
  }
  if (bid < 96) {
    float acc[8] = {0.f, 0.f, 0.f, 0.f, 0.f, 0.f, 0.f, 0.f};
#pragma unroll
    for (int i = 0; i < 4; ++i) {
      const float cf = bflo(cv[i]);
      const float sl = bfr(cf / (1.f + __expf(-cf)));
#pragma unroll
      for (int q = 0; q < 4; ++q) { acc[2 * q] = fmaf(sl, bflo(wm[i][q]), acc[2 * q]); acc[2 * q + 1] = fmaf(sl, bfhi(wm[i][q]), acc[2 * q + 1]); }
    }
#pragma unroll
    for (int q = 0; q < 8; ++q) {
#pragma unroll
      for (int o = 4; o <= 32; o <<= 1) acc[q] += __shfl_xor(acc[q], o);
    }
    if (lane < 4) {
#pragma unroll
      for (int q = 0; q < 8; ++q) red[w][lane * 8 + q] = acc[q];
    }
    __syncthreads();
    if (tid < 32) {
      float s = 0.f;
#pragma unroll
      for (int i = 0; i < 8; ++i) s += red[i][tid];
      ((float*)(ws + WS_MOD))[bid * 32 + tid] = bfr(s);
    }
  }
}

__global__ void __launch_bounds__(512, 2) k1_qkv(const u16* __restrict__ x, unsigned char* __restrict__ ws) {
  __shared__ __attribute__((aligned(16))) unsigned char lds[65536];
  LAS unsigned char* hb = (LAS unsigned char*)lds;
  const int tid = threadIdx.x, lane = tid & 63, w = __builtin_amdgcn_readfirstlane(tid >> 6);
  int rb, m; block_map(blockIdx.x, rb, m);
  const int g = lane >> 4, c = lane & 15;
  u32x4 xr[8];
#pragma unroll
  for (int i = 0; i < 8; ++i) xr[i] = *(const u32x4*)(x + (size_t)(64 * rb + 8 * w + i) * D_ + lane * 8);
  const float* mod = (const float*)(ws + WS_MOD);
  float sc[8], sh[8];
  {
    const f32x4 a0 = *(const f32x4*)(mod + lane * 8), a1 = *(const f32x4*)(mod + lane * 8 + 4);
    const f32x4 b0 = *(const f32x4*)(mod + 512 + lane * 8), b1 = *(const f32x4*)(mod + 512 + lane * 8 + 4);
#pragma unroll
    for (int q = 0; q < 4; ++q) { sh[q] = a0[q]; sh[4 + q] = a1[q]; sc[q] = bfr(1.f + b0[q]); sc[4 + q] = bfr(1.f + b1[q]); }
  }
  const int head = 4 * m + (w >> 1), rt = w & 1;
  u32x4 wf[16];
#pragma unroll
  for (int ks = 0; ks < 16; ++ks) wf[ks] = ldg16(ws + WS_WQ + (head * 16 + ks) * 1024 + lane * 16);
  ln_mod_store(xr, sc, sh, hb, 8 * w, lane);
  __syncthreads();
  f32x4 acc0 = {0.f, 0.f, 0.f, 0.f}, acc1 = acc0;
  const int r0 = 32 * rt + c;
#pragma unroll
  for (int ks = 0; ks < 16; ++ks) {
    const u32x4 b0 = *(const LAS u32x4*)(hb + r0 * 1024 + (((4 * ks + g) ^ c) << 4));
    const u32x4 b1 = *(const LAS u32x4*)(hb + (r0 + 16) * 1024 + (((4 * ks + g) ^ c) << 4));
    acc0 = mfma16(wf[ks], b0, acc0);
    acc1 = mfma16(wf[ks], b1, acc1);
  }
  {
    unsigned char* qb = ws + WS_QB + ((rb * 2 + rt) * 32 + head) * 1024 + ((g >> 1) * 32 + c) * 16 + (g & 1) * 8;
    u32x2 o0 = {pack2(acc0[0] * QSCALE, acc0[1] * QSCALE), pack2(acc0[2] * QSCALE, acc0[3] * QSCALE)};
    u32x2 o1 = {pack2(acc1[0] * QSCALE, acc1[1] * QSCALE), pack2(acc1[2] * QSCALE, acc1[3] * QSCALE)};
    *(u32x2*)qb = o0;
    *(u32x2*)(qb + 256) = o1;
  }
  if (w == 0) {
    const int sub = m >> 1, isv = m & 1;
    f32x4 a = {0.f, 0.f, 0.f, 0.f};
    const unsigned char* wp = ws + (isv ? WS_WV : WS_WK) + lane * 16;
#pragma unroll
    for (int ks = 0; ks < 16; ++ks) wf[ks] = ldg16(wp + ks * 1024);
#pragma unroll
    for (int ks = 0; ks < 16; ++ks) {
      const u32x4 hf = *(const LAS u32x4*)(hb + (16 * sub + c) * 1024 + (((4 * ks + g) ^ c) << 4));
      a = isv ? mfma16(hf, wf[ks], a) : mfma16(wf[ks], hf, a);
    }
    const u32x2 o = {pack2(a[0], a[1]), pack2(a[2], a[3])};
    const int tile = 2 * rb + (sub >> 1), s = sub & 1;
    unsigned char* kv = ws + WS_KV + tile * 2048;
    if (isv) {
      *(u32x2*)(kv + 1024 + s * 512 + (g & 1) * 256 + c * 16 + (g >> 1) * 8) = o;
    } else {
      *(u32x2*)(kv + ((g >> 1) * 32 + 16 * s + c) * 16 + (g & 1) * 8) = o;
      float n2 = dot2bf(o[0], o[0], dot2bf(o[1], o[1], 0.f));
      n2 += __shfl_xor(n2, 16);
      n2 += __shfl_xor(n2, 32);
      n2 = wave_max(n2);
      if (lane == 0) ((float*)(ws + WS_KMAX))[rb * 4 + sub] = n2;
    }
  }
}

constexpr int LDS_ONES = 131072;
constexpr int LDS_K2 = 131072 + 4096;

__device__ __forceinline__ void wait_flags(const unsigned char* ws, int kind, int first, int n, int lane) {
  const GAS unsigned* f = (const GAS unsigned*)(ws + WS_FLAG + (kind * 256 + first + (lane < n ? lane : 0)) * 128);
  for (int spins = 0; spins < (1 << 18); ++spins) {
    const unsigned v = __hip_atomic_load(f, RLX_AGENT);
    if (__all(v == 1u)) break;
    __builtin_amdgcn_s_sleep(1);
  }
  __builtin_amdgcn_fence(__ATOMIC_ACQUIRE, "wavefront");
}
__device__ __forceinline__ void publish(unsigned char* ws, int kind, int id, int tid) {
  DRAIN();
  __syncthreads();
  if (tid == 0) __hip_atomic_store((GAS unsigned*)(ws + WS_FLAG + (kind * 256 + id) * 128), 1u, RLX_AGENT);
}

__global__ void __launch_bounds__(512, 2) k2_main(const u16* __restrict__ x, unsigned char* __restrict__ ws, u16* __restrict__ out) {
  __shared__ __attribute__((aligned(16))) unsigned char lds[LDS_K2];
  LAS unsigned char* L = (LAS unsigned char*)lds;
  const int tid = threadIdx.x, lane = tid & 63, w = __builtin_amdgcn_readfirstlane(tid >> 6);
  int rb, m; block_map(blockIdx.x, rb, m);
  const int g = lane >> 4, c = lane & 15, hi = lane >> 5, c32 = lane & 31;
  const __amdgpu_buffer_rsrc_t rs = __builtin_amdgcn_make_buffer_rsrc(ws, 0, WS_END, 0x00020000);
  const float* mod = (const float*)(ws + WS_MOD);
  const unsigned lds0 = (unsigned)(uintptr_t)lds;

#pragma unroll
  for (int i = 0; i < 16; ++i) glds16(ws + WS_KV + (i * 8 + w) * 1024 + lane * 16, lds0 + (i * 8 + w) * 1024);
  *(LAS u32x2*)(L + LDS_ONES + tid * 8) = (u32x2){0x3f803f80u, 0x3f803f80u};
  const int head = 4 * m + (w >> 1), rt = w & 1;
  const u32x4 qf = ldg16(ws + WS_QB + ((rb * 2 + rt) * 32 + head) * 1024 + lane * 16);
  const float* kmx = (const float*)(ws + WS_KMAX);
  const float kmax2 = wave_max(fmaxf(kmx[lane], kmx[lane + 64]));
  float q2 = dot2bf(qf[0], qf[0], dot2bf(qf[1], qf[1], dot2bf(qf[2], qf[2], dot2bf(qf[3], qf[3], 0.f))));
  q2 += __shfl_xor(q2, 32);
  const float bound = __builtin_sqrtf(q2 * kmax2) * 1.0001f;
  DRAIN();
  __syncthreads();

  f32x16 o;
  {
    f32x16 cin;
#pragma unroll
    for (int r = 0; r < 16; ++r) { cin[r] = -bound; o[r] = 0.f; }
    const LAS unsigned char* kp = L + lane * 16;
    const LAS unsigned char* vp = (c32 < 16) ? (L + hi * 256 + c32 * 16) : (L + LDS_ONES - 1024 + (c32 - 16) * 16);
    const int vstep = (c32 < 16) ? 4096 : 0;
    f32x16 sA = mfma32(*(const LAS u32x4*)kp, qf, cin), sB;
    for (int t = 0; t < 64; t += 2) {
      const u32x4 kB = *(const LAS u32x4*)(kp + 2048);
      const u32x4 vA0 = *(const LAS u32x4*)(vp + 1024), vA1 = *(const LAS u32x4*)(vp + 1536);
      sB = mfma32(kB, qf, cin);
      u32x4 p0, p1;
#pragma unroll
      for (int q = 0; q < 4; ++q) {
        p0[q] = pack2(__builtin_amdgcn_exp2f(sA[2 * q]), __builtin_amdgcn_exp2f(sA[2 * q + 1]));
        p1[q] = pack2(__builtin_amdgcn_exp2f(sA[8 + 2 * q]), __builtin_amdgcn_exp2f(sA[8 + 2 * q + 1]));
      }
      o = mfma32(vA0, p0, o);
      o = mfma32(vA1, p1, o);
      const u32x4 kA = *(const LAS u32x4*)(kp + 4096);
      const u32x4 vB0 = *(const LAS u32x4*)(vp + 3072), vB1 = *(const LAS u32x4*)(vp + 3584);
      sA = mfma32(kA, qf, cin);
#pragma unroll
      for (int q = 0; q < 4; ++q) {
        p0[q] = pack2(__builtin_amdgcn_exp2f(sB[2 * q]), __builtin_amdgcn_exp2f(sB[2 * q + 1]));
        p1[q] = pack2(__builtin_amdgcn_exp2f(sB[8 + 2 * q]), __builtin_amdgcn_exp2f(sB[8 + 2 * q + 1]));
      }
      o = mfma32(vB0, p0, o);
      o = mfma32(vB1, p1, o);
      kp += 4096;
      vp += vstep;
    }
    if (!__all(o[8] >= 5.421010862427522e-20f)) {
      const LAS unsigned char* kq = L + lane * 16;
      const LAS unsigned char* vq = (c32 < 16) ? (L + hi * 256 + c32 * 16) : (L + LDS_ONES - 1024 + (c32 - 16) * 16);
      const int vs2 = (c32 < 16) ? 2048 : 0;
      f32x16 z;
#pragma unroll
      for (int r = 0; r < 16; ++r) { z[r] = 0.f; o[r] = 0.f; }
      float mrun = -INFINITY;
      for (int t = 0; t < 64; ++t) {
        const f32x16 s = mfma32(*(const LAS u32x4*)kq, qf, z);
        float tm = s[0];
#pragma unroll
        for (int r = 1; r < 16; ++r) tm = fmaxf(tm, s[r]);
        tm = fmaxf(tm, __shfl_xor(tm, 32));
        const float mn = fmaxf(mrun, tm);
        const float al = __builtin_amdgcn_exp2f(mrun - mn);
        mrun = mn;
        u32x4 p0, p1;
#pragma unroll
        for (int q = 0; q < 4; ++q) {
          p0[q] = pack2(__builtin_amdgcn_exp2f(s[2 * q] - mn), __builtin_amdgcn_exp2f(s[2 * q + 1] - mn));
          p1[q] = pack2(__builtin_amdgcn_exp2f(s[8 + 2 * q] - mn), __builtin_amdgcn_exp2f(s[8 + 2 * q + 1] - mn));
        }
#pragma unroll
        for (int r = 0; r < 16; ++r) o[r] *= al;
        o = mfma32(*(const LAS u32x4*)(vq + 1024), p0, o);
        o = mfma32(*(const LAS u32x4*)(vq + 1536), p1, o);
        kq += 2048;
        vq += vs2;
      }
    }
  }
  {
    const float rl = 1.f / o[8];
    u32x4 of;
#pragma unroll
    for (int q = 0; q < 4; ++q) of[q] = pack2(o[2 * q] * rl, o[2 * q + 1] * rl);
    __builtin_amdgcn_raw_buffer_store_b128(of, rs, WS_OB + ((rb * 2 + rt) * 32 + head) * 1024 + lane * 16, 0, AUX_SC1);
  }
  publish(ws, 0, rb * 8 + m, tid);

  const int rs4 = w & 3, kh = w >> 2;
  const int erow = 64 * rb + 16 * rs4 + c, ecol = 64 * m + 32 * kh + 8 * g;
  float x1[8];
  {
    u32x4 wf[32];
#pragma unroll
    for (int i = 0; i < 32; ++i) wf[i] = ldg16(ws + WS_WO + ((m * 16 + 8 * kh) * 4 + i) * 1024 + lane * 16);
    const u32x4 xin = *(const u32x4*)(x + (size_t)erow * D_ + ecol);
    const f32x4 g0 = *(const f32x4*)(mod + 1024 + ecol), g1 = *(const f32x4*)(mod + 1024 + ecol + 4);
    wait_flags(ws, 0, rb * 8 + 4 * kh, 4, lane);
    u32x4 bf[8];
#pragma unroll
    for (int i = 0; i < 8; ++i)
      bf[i] = __builtin_amdgcn_raw_buffer_load_b128(
          rs, WS_OB + ((rb * 2 + (rs4 >> 1)) * 32 + 2 * (8 * kh + i) + (g >> 1)) * 1024 + ((g & 1) * 32 + 16 * (rs4 & 1) + c) * 16, 0, AUX_SC1);
    f32x4 acc[4];
#pragma unroll
    for (int t = 0; t < 4; ++t) acc[t] = (f32x4){0.f, 0.f, 0.f, 0.f};
#pragma unroll
    for (int i = 0; i < 8; ++i)
#pragma unroll
      for (int t = 0; t < 4; ++t) acc[t] = mfma16(wf[i * 4 + t], bf[i], acc[t]);
    LAS f32x4* ex = (LAS f32x4*)L;
    ex[(w * 2 + 0) * 64 + lane] = kh ? acc[0] : acc[2];
    ex[(w * 2 + 1) * 64 + lane] = kh ? acc[1] : acc[3];
    __syncthreads();
    const f32x4 r0 = ex[((w ^ 4) * 2 + 0) * 64 + lane], r1 = ex[((w ^ 4) * 2 + 1) * 64 + lane];
    const f32x4 a0 = (kh ? acc[2] : acc[0]) + r0, a1 = (kh ? acc[3] : acc[1]) + r1;
#pragma unroll
    for (int q = 0; q < 2; ++q) {
      x1[2 * q] = fmaf(g0[2 * q], a0[2 * q], bflo(xin[q]));
      x1[2 * q + 1] = fmaf(g0[2 * q + 1], a0[2 * q + 1], bfhi(xin[q]));
      x1[4 + 2 * q] = fmaf(g1[2 * q], a1[2 * q], bflo(xin[2 + q]));
      x1[4 + 2 * q + 1] = fmaf(g1[2 * q + 1], a1[2 * q + 1], bfhi(xin[2 + q]));
    }
    u32x4 xo;
#pragma unroll
    for (int q = 0; q < 4; ++q) { xo[q] = pack2(x1[2 * q], x1[2 * q + 1]); x1[2 * q] = bflo(xo[q]); x1[2 * q + 1] = bfhi(xo[q]); }
    __builtin_amdgcn_raw_buffer_store_b128(xo, rs, WS_X1 + erow * 1024 + ecol * 2, 0, AUX_SC1);
  }
  publish(ws, 1, rb * 8 + m, tid);

  LAS unsigned char* hb = L + 65536;
  {
    u32x4 wf[32];
    const int nt = 7 * m + (w < 7 ? w : 0);
#pragma unroll
    for (int i = 0; i < 32; ++i) wf[i] = ldg16(ws + WS_W1 + (nt * 32 + i) * 1024 + lane * 16);
    float sc[8], sh[8];
    {
      const f32x4 a0 = *(const f32x4*)(mod + 1536 + lane * 8), a1 = *(const f32x4*)(mod + 1536 + lane * 8 + 4);
      const f32x4 b0 = *(const f32x4*)(mod + 2048 + lane * 8), b1 = *(const f32x4*)(mod + 2048 + lane * 8 + 4);
#pragma unroll
      for (int q = 0; q < 4; ++q) { sh[q] = a0[q]; sh[4 + q] = a1[q]; sc[q] = bfr(1.f + b0[q]); sc[4 + q] = bfr(1.f + b1[q]); }
    }
    wait_flags(ws, 1, rb * 8, 8, lane);
    u32x4 xr[8];
#pragma unroll
    for (int i = 0; i < 8; ++i) xr[i] = __builtin_amdgcn_raw_buffer_load_b128(rs, WS_X1 + (64 * rb + 8 * w + i) * 1024 + lane * 16, 0, AUX_SC1);
    ln_mod_store(xr, sc, sh, hb, 8 * w, lane);
    __syncthreads();
    if (w < 7) {
#pragma unroll
      for (int r2 = 0; r2 < 2; ++r2) {
        f32x16 acc;
#pragma unroll
        for (int r = 0; r < 16; ++r) acc[r] = 0.f;
#pragma unroll
        for (int ks = 0; ks < 32; ++ks) {
          const u32x4 b = *(const LAS u32x4*)(hb + (32 * r2 + c32) * 1024 + (((2 * ks + hi) ^ (c32 & 15)) << 4));
          acc = mfma32(wf[ks], b, acc);
        }
        u32x4 h0, h1;
#pragma unroll
        for (int r = 0; r < 16; ++r) {
          const float v = bfr(acc[r]);
          acc[r] = v * __builtin_amdgcn_rcpf(1.f + __builtin_amdgcn_exp2f(v * -1.4426950408889634f));
        }
#pragma unroll
        for (int q = 0; q < 4; ++q) { h0[q] = pack2(acc[2 * q], acc[2 * q + 1]); h1[q] = pack2(acc[8 + 2 * q], acc[8 + 2 * q + 1]); }
        const int ho = WS_HB + ((rb * 2 + r2) * 112 + 2 * nt) * 1024 + lane * 16;
        __builtin_amdgcn_raw_buffer_store_b128(h0, rs, ho, 0, AUX_SC1);
        __builtin_amdgcn_raw_buffer_store_b128(h1, rs, ho + 1024, 0, AUX_SC1);
      }
    }
  }
  publish(ws, 2, rb * 8 + m, tid);

  {
    u32x4 wf[28];
#pragma unroll
    for (int i = 0; i < 28; ++i) wf[i] = ldg16(ws + WS_W2 + ((m * 112 + 14 * w) * 2 + i) * 1024 + lane * 16);
    const f32x4 g0 = *(const f32x4*)(mod + 2560 + ecol), g1 = *(const f32x4*)(mod + 2560 + ecol + 4);
    wait_flags(ws, 2, rb * 8 + w, 1, lane);
    f32x16 acc[2][2];
#pragma unroll
    for (int r = 0; r < 16; ++r) { acc[0][0][r] = 0.f; acc[0][1][r] = 0.f; acc[1][0][r] = 0.f; acc[1][1][r] = 0.f; }
#pragma unroll
    for (int r2 = 0; r2 < 2; ++r2) {
      u32x4 hf[14];
#pragma unroll
      for (int i = 0; i < 14; ++i)
        hf[i] = __builtin_amdgcn_raw_buffer_load_b128(rs, WS_HB + ((rb * 2 + r2) * 112 + 14 * w + i) * 1024 + lane * 16, 0, AUX_SC1);
#pragma unroll
      for (int i = 0; i < 14; ++i) {
        acc[0][r2] = mfma32(wf[2 * i], hf[i], acc[0][r2]);
        acc[1][r2] = mfma32(wf[2 * i + 1], hf[i], acc[1][r2]);
      }
    }
#pragma unroll
    for (int ct = 0; ct < 2; ++ct)
#pragma unroll
      for (int r2 = 0; r2 < 2; ++r2)
#pragma unroll
        for (int q = 0; q < 4; ++q) {
          const f32x4 v = {acc[ct][r2][4 * q], acc[ct][r2][4 * q + 1], acc[ct][r2][4 * q + 2], acc[ct][r2][4 * q + 3]};
          *(LAS f32x4*)(L + w * 16384 + (32 * r2 + c32) * 256 + (((8 * ct + 4 * hi + q) ^ (c32 & 15)) << 4)) = v;
        }
    __syncthreads();
    f32x4 a0 = {0.f, 0.f, 0.f, 0.f}, a1 = a0;
#pragma unroll
    for (int p = 0; p < 8; ++p) {
      a0 += *(const LAS f32x4*)(L + p * 16384 + (16 * rs4 + c) * 256 + (((8 * kh + 2 * g) ^ c) << 4));
      a1 += *(const LAS f32x4*)(L + p * 16384 + (16 * rs4 + c) * 256 + (((8 * kh + 2 * g + 1) ^ c) << 4));
    }
    u32x4 xo;
    xo[0] = pack2(fmaf(g0[0], a0[0], x1[0]), fmaf(g0[1], a0[1], x1[1]));
    xo[1] = pack2(fmaf(g0[2], a0[2], x1[2]), fmaf(g0[3], a0[3], x1[3]));
    xo[2] = pack2(fmaf(g1[0], a1[0], x1[4]), fmaf(g1[1], a1[1], x1[5]));
    xo[3] = pack2(fmaf(g1[2], a1[2], x1[6]), fmaf(g1[3], a1[3], x1[7]));
    *(u32x4*)(out + (size_t)erow * D_ + ecol) = xo;
  }
}

extern "C" void kernel_launch(void* const* d_in, const int* in_sizes, int n_in, void* d_out, int out_size, void* d_ws, size_t ws_size,
                              hipStream_t stream) {
  const u16* x = (const u16*)d_in[0];
  unsigned char* ws = (unsigned char*)d_ws;
  k0_prep<<<256, 512, 0, stream>>>((const u16*)d_in[1], (const u16*)d_in[2], (const u16*)d_in[3], (const u16*)d_in[4], (const u16*)d_in[6],
                                   (const u16*)d_in[7], (const u16*)d_in[8], (const u16*)d_in[9], ws);
  k1_qkv<<<256, 512, 0, stream>>>(x, ws);
  k2_main<<<256, 512, 0, stream>>>(x, ws, (u16*)d_out);
}
```

```cpp
#include <hip/hip_runtime.h>
#include <hip/hip_bf16.h>
#include <stdint.h>

typedef __bf16 bf16x8 __attribute__((ext_vector_type(8)));
typedef __bf16 bf16x2 __attribute__((ext_vector_type(2)));
typedef float f32x2 __attribute__((ext_vector_type(2)));
typedef float f32x4 __attribute__((ext_vector_type(4)));
typedef float f32x16 __attribute__((ext_vector_type(16)));
typedef unsigned u32x4 __attribute__((ext_vector_type(4)));
typedef unsigned u32x2 __attribute__((ext_vector_type(2)));
typedef unsigned u32x16 __attribute__((ext_vector_type(16)));
typedef short s16x4 __attribute__((ext_vector_type(4)));
typedef unsigned short u16;
typedef unsigned long long u64;
#define LAS __attribute__((address_space(3)))
#define GAS __attribute__((address_space(1)))
#define RLX_AGENT __ATOMIC_RELAXED, __HIP_MEMORY_SCOPE_AGENT
#define SBAR() __builtin_amdgcn_sched_barrier(0)
#define PIN(x) asm volatile("" : "+v"(x))
#define DRAIN() asm volatile("s_waitcnt vmcnt(0)" ::: "memory")
#define CFENCE() asm volatile("" ::: "memory")

constexpr int D_ = 512, F_ = 1792;
constexpr float QSCALE = 0.25f * 1.4426950408889634f;
constexpr float LN_EPS = 1e-5f;
constexpr int AUX_SC1 = 16;
constexpr unsigned SENT = 0xffffffffu;
constexpr int SPIN_CAP = 1 << 17;

constexpr int WS_EPOCH = 0;
constexpr int WS_AUXF = 1024;
constexpr int WS_FLAG = 16384;
constexpr int WS_MODP = 131072;
constexpr int WS_KV = 262144;
constexpr int WS_MODG = 393216;
constexpr int WS_OB = 1310720;
constexpr int WS_ST = 917504;
constexpr int WS_YB = WS_OB + 2097152;
constexpr int WS_HB = WS_YB + 2097152;
constexpr int WS_WO = WS_HB + 7340032;
constexpr int WS_W1 = WS_WO + 512 * 1024;
constexpr int WS_W2 = WS_W1 + 1792 * 1024;
constexpr int WS_END = WS_W2 + 1792 * 1024;
#define STAMP(i)
#define STAMPW(i)

constexpr int HROW = 1040;
constexpr int L_H1 = 65536;
constexpr int L_ONES = 132096;
constexpr int L_WKV = 140288;
constexpr int L_MODA = 156672;
constexpr int L_RED = 158720;
constexpr int L_CNT = 160768;
constexpr int L_END = 160896;
constexpr int L_W2 = 65536;
constexpr int L_TAB = L_MODA;
constexpr int L_STAT = 155648;
#define PRIO0 3
#define PRIO1 2
#define PRIO2 1
#define PRIO3 0
#define PRIO4 3
#define PRIO5 2
#define PRIO6 1
#define PRIO7 0

__device__ __forceinline__ unsigned pack2(float a, float b) {
  f32x2 v = {a, b};
  return __builtin_bit_cast(unsigned, __builtin_convertvector(v, bf16x2));
}
__device__ __forceinline__ float bflo(unsigned u) { return __uint_as_float(u << 16); }
__device__ __forceinline__ float bfhi(unsigned u) { return __uint_as_float(u & 0xffff0000u); }
__device__ __forceinline__ float bfr(float a) { return bflo(pack2(a, 0.f)); }
__device__ __forceinline__ f32x16 mfma32(u32x4 a, u32x4 b, f32x16 c) {
  return __builtin_amdgcn_mfma_f32_32x32x16_bf16(__builtin_bit_cast(bf16x8, a), __builtin_bit_cast(bf16x8, b), c, 0, 0, 0);
}
__device__ __forceinline__ f32x4 mfma16(u32x4 a, u32x4 b, f32x4 c) {
  return __builtin_amdgcn_mfma_f32_16x16x32_bf16(__builtin_bit_cast(bf16x8, a), __builtin_bit_cast(bf16x8, b), c, 0, 0, 0);
}
__device__ __forceinline__ float dot2bf(unsigned a, unsigned b, float c) {
  return __builtin_amdgcn_fdot2_f32_bf16(__builtin_bit_cast(bf16x2, a), __builtin_bit_cast(bf16x2, b), c, false);
}
template <int CTRL> __device__ __forceinline__ float dpp(float v) {
  return __int_as_float(__builtin_amdgcn_update_dpp(0, __float_as_int(v), CTRL, 0xf, 0xf, true));
}
__device__ __forceinline__ float row16_sum(float t) { t += dpp<0xB1>(t); t += dpp<0x4E>(t); t += dpp<0x141>(t); t += dpp<0x140>(t); return t; }
__device__ __forceinline__ float row16_max(float t) { t = fmaxf(t, dpp<0xB1>(t)); t = fmaxf(t, dpp<0x4E>(t)); t = fmaxf(t, dpp<0x141>(t)); t = fmaxf(t, dpp<0x140>(t)); return t; }
__device__ __forceinline__ float fold32(float a, float b) {
  auto r = __builtin_amdgcn_permlane32_swap(__float_as_uint(a), __float_as_uint(b), false, false);
  return __uint_as_float(r[0]) + __uint_as_float(r[1]);
}
__device__ __forceinline__ float fold16(float a, float b) {
  auto r = __builtin_amdgcn_permlane16_swap(__float_as_uint(a), __float_as_uint(b), false, false);
  return __uint_as_float(r[0]) + __uint_as_float(r[1]);
}
__device__ __forceinline__ float max32(float a) {
  auto r = __builtin_amdgcn_permlane32_swap(__float_as_uint(a), __float_as_uint(a), false, false);
  return fmaxf(__uint_as_float(r[0]), __uint_as_float(r[1]));
}
__device__ __forceinline__ float max16(float a) {
  auto r = __builtin_amdgcn_permlane16_swap(__float_as_uint(a), __float_as_uint(a), false, false);
  return fmaxf(__uint_as_float(r[0]), __uint_as_float(r[1]));
}
__device__ __forceinline__ float rdlane(float v, int l) { return __int_as_float(__builtin_amdgcn_readlane(__float_as_int(v), l)); }
__device__ __forceinline__ void glds16(const void* g, unsigned lds_base) {
  unsigned sv;
  asm volatile("s_mov_b32 %0, m0\n\ts_mov_b32 m0, %2\n\ts_nop 0\n\tglobal_load_lds_dwordx4 %1, off sc1\n\ts_mov_b32 m0, %0"
               : "=&s"(sv) : "v"(g), "s"(lds_base) : "memory");
}
__device__ __forceinline__ u32x4 ldg16(const void* p) { return *(const GAS u32x4*)p; }
__device__ __forceinline__ u32x2 tr_read(const LAS unsigned char* p) {
  return __builtin_bit_cast(u32x2, __builtin_amdgcn_ds_read_tr16_b64_v4i16((LAS s16x4*)p));
}
__device__ __forceinline__ int fresh_lane() {
  int l;
  asm volatile("v_mbcnt_lo_u32_b32 %0, -1, 0\n\tv_mbcnt_hi_u32_b32 %0, -1, %0" : "=v"(l));
  return l;
}
#define LD16(voff, soff) __builtin_amdgcn_raw_buffer_load_b128(rs, voff, soff, AUX_SC1)
#define LDF4(voff, soff) __builtin_bit_cast(f32x4, LD16(voff, soff))
#define LDMOD(voff, soff) ({ const f32x4 t_ = LDF4(voff, WS_MODP + (soff)) + LDF4(voff, WS_MODP + 8192 + (soff)); \
    const unsigned a_ = pack2(t_[0], t_[1]), b_ = pack2(t_[2], t_[3]); (f32x4){bflo(a_), bfhi(a_), bflo(b_), bfhi(b_)}; })
#define ST16(v, voff, soff) do { const u32x4 d_ = (v); __builtin_amdgcn_raw_buffer_store_b128(d_, rs, voff, soff, AUX_SC1); asm volatile("s_nop 1" :: "v"(d_)); } while (0)
__device__ __forceinline__ unsigned umax4(u32x4 d) { return max(max(d[0], d[1]), max(d[2], d[3])); }

__device__ __forceinline__ void ln_stats(const u32x4 (&xr)[8], float (&rs)[8], float (&nb)[8]) {
  float s[8], q[8];
#pragma unroll
  for (int i = 0; i < 8; ++i) {
    s[i] = dot2bf(xr[i][0], 0x3f803f80u, dot2bf(xr[i][1], 0x3f803f80u, dot2bf(xr[i][2], 0x3f803f80u, dot2bf(xr[i][3], 0x3f803f80u, 0.f))));
    q[i] = dot2bf(xr[i][0], xr[i][0], dot2bf(xr[i][1], xr[i][1], dot2bf(xr[i][2], xr[i][2], dot2bf(xr[i][3], xr[i][3], 0.f))));
  }
  float ts[4], tq[4];
#pragma unroll
  for (int i = 0; i < 4; ++i) { ts[i] = fold32(s[i], s[i + 4]); tq[i] = fold32(q[i], q[i + 4]); }
#pragma unroll
  for (int i = 0; i < 2; ++i) {
    const float mu = row16_sum(fold16(ts[i], ts[i + 2])) * (1.f / D_);
    const float e2 = row16_sum(fold16(tq[i], tq[i + 2])) * (1.f / D_);
    const float r = __builtin_amdgcn_rsqf(fmaxf(fmaf(-mu, mu, e2), 0.f) + LN_EPS);
    const float n = -mu * r;
#pragma unroll
    for (int p = 0; p < 4; ++p) { rs[i + 2 * p] = rdlane(r, 16 * p); nb[i + 2 * p] = rdlane(n, 16 * p); }
  }
}

#define SUB4(v, i) __builtin_shufflevector(v, v, 4 * (i), 4 * (i) + 1, 4 * (i) + 2, 4 * (i) + 3)
__device__ __forceinline__ unsigned lds_arrive(LAS unsigned char* p, int lane) {
  asm volatile("s_waitcnt lgkmcnt(0)" ::: "memory");
  unsigned old = 0;
  if (lane == 0) old = __hip_atomic_fetch_add((LAS unsigned*)p, 1u, __ATOMIC_RELAXED, __HIP_MEMORY_SCOPE_WORKGROUP);
  return __builtin_amdgcn_readfirstlane(old);
}
__device__ __forceinline__ void lds_wait8(LAS unsigned char* p) {
  unsigned v, t, n;
  asm volatile("s_mov_b32 %2, 0x20000\n"
               "1:\nds_read_b32 %0, %3\ns_waitcnt lgkmcnt(0)\nv_readfirstlane_b32 %1, %0\ns_cmp_ge_u32 %1, 8\ns_cbranch_scc1 2f\n"
               "s_sleep 1\ns_sub_u32 %2, %2, 1\ns_cmp_lg_u32 %2, 0\ns_cbranch_scc1 1b\n2:\n"
               : "=&v"(v), "=&s"(t), "=&s"(n) : "v"((unsigned)(uintptr_t)p) : "memory", "scc");
}
__device__ __forceinline__ float knorm2(u32x4 k) { return dot2bf(k[0], k[0], dot2bf(k[1], k[1], dot2bf(k[2], k[2], dot2bf(k[3], k[3], 0.f)))); }

__device__ __forceinline__ void wait_flags(const unsigned char* ws, int kind, int first, int n, int lane) {
  const GAS unsigned* f = (const GAS unsigned*)(ws + WS_FLAG + (kind * 256 + first + (lane < n ? lane : 0)) * 128);
#pragma unroll 1
  for (int spins = 0; spins < SPIN_CAP; ++spins) {
    const unsigned v = __hip_atomic_load(f, RLX_AGENT);
    if (__all(v == 1u)) break;
    __builtin_amdgcn_s_sleep(1);
  }
  __builtin_amdgcn_fence(__ATOMIC_ACQUIRE, "wavefront");
}
__device__ __forceinline__ void publish(unsigned char* ws, int off, int tid) {
  DRAIN();
  __syncthreads();
  if (tid == 0) __hip_atomic_store((GAS unsigned*)(ws + off), 1u, RLX_AGENT);
}

#define ATT_TILE_A(KO, V1O, V0O, ADV, X1, X2) \
  "s_waitcnt lgkmcnt(0)\nv_mfma_f32_32x32x16_bf16 v[208:223], v[228:231], v[224:227], v[160:175]\n" \
  "ds_read_b128 v[228:231], v248 offset:" KO "\n" \
  "v_exp_f32 v192, v192\nv_exp_f32 v193, v193\nv_exp_f32 v194, v194\nv_cvt_pk_bf16_f32 v240, v192, v193\n" \
  X1 \
  "v_exp_f32 v195, v195\nv_exp_f32 v196, v196\nv_cvt_pk_bf16_f32 v241, v194, v195\nv_exp_f32 v197, v197\n" \
  "v_mfma_f32_32x32x16_bf16 v[176:191], v[236:239], v[244:247], v[176:191]\n" \
  "ds_read_b128 v[236:239], v249 offset:" V1O "\n" \
  ADV \
  "v_exp_f32 v198, v198\nv_cvt_pk_bf16_f32 v242, v196, v197\nv_exp_f32 v199, v199\nv_exp_f32 v200, v200\n" \
  "v_cvt_pk_bf16_f32 v243, v198, v199\nv_exp_f32 v201, v201\nv_exp_f32 v202, v202\nv_cvt_pk_bf16_f32 v244, v200, v201\n" \
  "v_mfma_f32_32x32x16_bf16 v[176:191], v[232:235], v[240:243], v[176:191]\n" \
  "ds_read_b128 v[232:235], v249 offset:" V0O "\n" \
  "v_exp_f32 v203, v203\nv_exp_f32 v204, v204\nv_exp_f32 v205, v205\nv_cvt_pk_bf16_f32 v245, v202, v203\n" \
  X2 \
  "v_exp_f32 v206, v206\nv_exp_f32 v207, v207\nv_cvt_pk_bf16_f32 v246, v204, v205\nv_cvt_pk_bf16_f32 v247, v206, v207\n"
#define ATT_TILE_B(KO, V1O, V0O, ADV, X1, X2) \
  "s_waitcnt lgkmcnt(0)\nv_mfma_f32_32x32x16_bf16 v[192:207], v[228:231], v[224:227], v[160:175]\n" \
  "ds_read_b128 v[228:231], v248 offset:" KO "\n" \
  "v_exp_f32 v208, v208\nv_exp_f32 v209, v209\nv_exp_f32 v210, v210\nv_cvt_pk_bf16_f32 v240, v208, v209\n" \
  X1 \
  "v_exp_f32 v211, v211\nv_exp_f32 v212, v212\nv_cvt_pk_bf16_f32 v241, v210, v211\nv_exp_f32 v213, v213\n" \
  "v_mfma_f32_32x32x16_bf16 v[176:191], v[236:239], v[244:247], v[176:191]\n" \
  "ds_read_b128 v[236:239], v249 offset:" V1O "\n" \
  ADV \
  "v_exp_f32 v214, v214\nv_cvt_pk_bf16_f32 v242, v212, v213\nv_exp_f32 v215, v215\nv_exp_f32 v216, v216\n" \
  "v_cvt_pk_bf16_f32 v243, v214, v215\nv_exp_f32 v217, v217\nv_exp_f32 v218, v218\nv_cvt_pk_bf16_f32 v244, v216, v217\n" \
  "v_mfma_f32_32x32x16_bf16 v[176:191], v[232:235], v[240:243], v[176:191]\n" \
  "ds_read_b128 v[232:235], v249 offset:" V0O "\n" \
  "v_exp_f32 v219, v219\nv_exp_f32 v220, v220\nv_exp_f32 v221, v221\nv_cvt_pk_bf16_f32 v245, v218, v219\n" \
  X2 \
  "v_exp_f32 v222, v222\nv_exp_f32 v223, v223\nv_cvt_pk_bf16_f32 v246, v220, v221\nv_cvt_pk_bf16_f32 v247, v222, v223\n"
#define ATT_ASM(a0, b0, a1, b1, a2, b2, a3, b3, a4, b4, a5, b5, a6, b6, a7, b7) \
  "s_nop 1\n" \
  ATT_TILE_A("2048", "512", "1024", "", a0, b0) \
  ATT_TILE_B("3072", "1536", "2048", "", a1, b1) \
  ATT_TILE_A("4096", "2560", "3072", "", a2, b2) \
  ATT_TILE_B("5120", "3584", "4096", "", a3, b3) \
  ATT_TILE_A("6144", "4608", "5120", "", a4, b4) \
  ATT_TILE_B("7168", "5632", "6144", "", a5, b5) \
  ATT_TILE_A("16384", "6656", "7168", "", a6, b6) \
  ATT_TILE_B("17408", "7680", "0", "v_add_u32 v249, v249, v250\nv_add_u32 v248, 0x4000, v248\n", a7, b7) \
  "s_waitcnt lgkmcnt(0)\ns_nop 3\n"

#define ATT_STATE "+{v[176:191]}"(o), "+{v[192:207]}"(sA), "+{v[228:231]}"(kf), "+{v[232:235]}"(v0), "+{v[236:239]}"(v1), \
                  "+{v[244:247]}"(p1), "+{v248}"(kp), "+{v249}"(vp)
#define ATT_IN "{v[160:175]}"(cin), "{v[224:227]}"(qf), "{v250}"(vstep)
#define ATT_CLOB "v208", "v209", "v210", "v211", "v212", "v213", "v214", "v215", "v216", "v217", "v218", "v219", "v220", "v221", "v222", \
                 "v223", "v240", "v241", "v242", "v243", "memory"
#define ATT_PLAIN() do { SBAR(); asm volatile(ATT_ASM("", "", "", "", "", "", "", "", "", "", "", "", "", "", "", "") \
    : ATT_STATE : ATT_IN : ATT_CLOB); SBAR(); } while (0)
#define GL(R, VO, OFF) "global_load_dwordx4 " R ", " VO ", %[sb] offset:" OFF " sc1\n"
#define DMA(VO, OFF) "global_load_lds_dwordx4 " VO ", %[sb] offset:" OFF " sc1\n"

__global__ void __launch_bounds__(512, 2)
fused_block(const u16* __restrict__ x, const u16* __restrict__ wq, const u16* wk, const u16* wv,
            const u16* __restrict__ wo, const u16* __restrict__ w1, const u16* __restrict__ w2, const u16* cvec,
            const u16* wmod, unsigned char* ws, u16* __restrict__ out) {
  __shared__ __attribute__((aligned(16))) unsigned char lds[L_END];
  LAS unsigned char* L = (LAS unsigned char*)lds;
  const unsigned lds0 = (unsigned)(uintptr_t)lds;
  const int bid = blockIdx.x, tid = threadIdx.x, lane = tid & 63, w = __builtin_amdgcn_readfirstlane(tid >> 6);
  const int rb = (bid & 7) * 4 + ((bid >> 3) & 3), m = bid >> 5;
  const int g = lane >> 4, c = lane & 15, hi = lane >> 5, c32 = lane & 31;
  const __amdgpu_buffer_rsrc_t rs = __builtin_amdgcn_make_buffer_rsrc(ws, 0, WS_END, 0x00020000);
  STAMP(0);

  const bool crit = bid < 64;
  if (crit) {
    if (w < 2) ST16(((u32x4){SENT, SENT, SENT, SENT}), tid * 16, WS_KV + bid * 2048);
    if (w == 2 && lane < 16)
      __builtin_amdgcn_raw_buffer_store_b32(0u, rs, lane < 12 ? WS_FLAG + (bid * 12 + lane) * 128 : WS_AUXF + (bid * 4 + lane - 12) * 4, 0, AUX_SC1);
    CFENCE();
  }
  if (tid < 8) *(LAS unsigned*)(L + L_CNT + tid * 4) = 0u;
  const u64 epoch = *(const __attribute__((address_space(4))) u64*)(ws + WS_EPOCH) + 0x9E3779B97F4A7C15ull;
  const bool late = (bid >> 6) == 1, kvwg = bid >= 128;
  u32x4 wm[4];
  u16 cv[4];
  const int mvo = (tid >> 3) * 6144 + (tid & 7) * 16;
  {
    const __amdgpu_buffer_rsrc_t ms = __builtin_amdgcn_make_buffer_rsrc((void*)wmod, 0, crit ? 512 * 3072 * 2 : 0, 0x00020000);
    const __amdgpu_buffer_rsrc_t cs = __builtin_amdgcn_make_buffer_rsrc((void*)cvec, 0, crit ? 512 * 2 : 0, 0x00020000);
    const int so = (bid >> 4) * 128 * 6144 + (bid & 15) * 128;
#pragma unroll
    for (int i = 0; i < 2; ++i) {
      wm[i] = __builtin_amdgcn_raw_buffer_load_b128(ms, mvo, so + i * 64 * 6144, 0);
      cv[i] = __builtin_amdgcn_raw_buffer_load_b16(cs, (tid >> 3) * 2, (bid >> 4) * 256 + i * 128, 0);
    }
    CFENCE();
  }
#define XROW(i) (16 * ((((i) >> 1) + m) & 3) + 2 * w + ((i) & 1))
  u32x4 xr[8];
#pragma unroll
  for (int i = 0; i < 4; ++i) xr[i] = ldg16(x + (size_t)(64 * rb + XROW(i)) * D_ + lane * 8);
  SBAR();
#define SILU(v) ((v) * __builtin_amdgcn_rcpf(1.f + __builtin_amdgcn_exp2f((v) * -1.4426950408889634f)))
#define MOD_PART(N) do { \
    float p[8] = {0.f, 0.f, 0.f, 0.f, 0.f, 0.f, 0.f, 0.f}; \
    _Pragma("unroll") for (int i = 0; i < N; ++i) { const float sl = bfr(SILU(bflo(cv[i]))); \
      _Pragma("unroll") for (int q = 0; q < 4; ++q) { p[2 * q] = fmaf(sl, bflo(wm[i][q]), p[2 * q]); p[2 * q + 1] = fmaf(sl, bfhi(wm[i][q]), p[2 * q + 1]); } } \
    float t0 = fold16(fold32(p[0], p[4]), fold32(p[2], p[6])), t1 = fold16(fold32(p[1], p[5]), fold32(p[3], p[7])); \
    t0 += dpp<0x128>(t0); t1 += dpp<0x128>(t1); \
    if (c < 8) *(LAS f32x2*)(L + L_RED + (w * 64 + c * 8 + 2 * g) * 4) = (f32x2){t0, t1}; } while (0)
#define MOD_SUM() ({ float s_ = 0.f; _Pragma("unroll") for (int i = 0; i < 8; ++i) s_ += *(const LAS float*)(L + L_RED + (i * 64 + lane) * 4); s_; })
  if (crit) {
    MOD_PART(2);
    __syncthreads();
    const float s = MOD_SUM();
    const u64 gr = (epoch & ~0xffffffull) | ((__float_as_uint(s) + 0x80u) >> 8);
    GAS u64* gp = (GAS u64*)(ws + WS_MODG + w * 32768 + (bid >> 4) * 8192 + ((bid & 15) * 64 + lane) * 8);
    __hip_atomic_store(gp, gr, RLX_AGENT);
    __hip_atomic_store(gp + 32768, gr, RLX_AGENT);
  }
  STAMP(1);
#pragma unroll
  for (int i = 4; i < 8; ++i) xr[i] = ldg16(x + (size_t)(64 * rb + XROW(i)) * D_ + lane * 8);
  u32x4 wst[8];
#pragma unroll
  for (int i = 0; i < 8; ++i) wst[i] = ldg16(wq + (size_t)(64 * ((i + 2 * (bid >> 3)) & 7) + (tid >> 3)) * D_ + 64 * m + (tid & 7) * 8);
  {
    const __amdgpu_buffer_rsrc_t a0 = __builtin_amdgcn_make_buffer_rsrc(late ? (void*)wmod : (void*)wk, 0, late ? 512 * 3072 * 2 : (kvwg ? 16384 : 0), 0x00020000);
    const __amdgpu_buffer_rsrc_t a1 = __builtin_amdgcn_make_buffer_rsrc(late ? (void*)wmod : (void*)wv, 0, late ? 512 * 3072 * 2 : (kvwg ? 16384 : 0), 0x00020000);
    const __amdgpu_buffer_rsrc_t cs = __builtin_amdgcn_make_buffer_rsrc((void*)cvec, 0, late ? 512 * 2 : 0, 0x00020000);
    const int vo = late ? mvo : w * 2048 + lane * 16;
    const int s0 = late ? (m & 1) * 256 * 6144 + 2048 + (bid & 31) * 128 : 0, sd = late ? 64 * 6144 : 1024, s2 = late ? s0 + 128 * 6144 : 0;
    wm[0] = __builtin_amdgcn_raw_buffer_load_b128(a0, vo, s0, 0);
    wm[1] = __builtin_amdgcn_raw_buffer_load_b128(a0, vo, s0 + sd, 0);
    wm[2] = __builtin_amdgcn_raw_buffer_load_b128(a1, vo, s2, 0);
    wm[3] = __builtin_amdgcn_raw_buffer_load_b128(a1, vo, s2 + sd, 0);
#pragma unroll
    for (int i = 0; i < 4; ++i) cv[i] = __builtin_amdgcn_raw_buffer_load_b16(cs, (tid >> 3) * 2, (m & 1) * 512 + i * 128, 0);
    CFENCE();
  }
  SBAR();
  STAMP(21);
  float rsd[8], nb[8];
  ln_stats(xr, rsd, nb);
  STAMP(22);
  const int hh = w >> 1, rt = w & 1;
  u32x4 kfr[4];
  {
    const int r0 = tid >> 3, f = ((r0 >> 1) & 1) | ((r0 >> 2) & 2);
#pragma unroll
    for (int i = 0; i < 8; ++i) *(LAS u32x4*)(L + (64 * ((i + 2 * (bid >> 3)) & 7) + r0) * 128 + (((tid & 7) ^ (f << 1)) << 4)) = wst[i];
    const unsigned ov = (c & 11) == 0 ? 0x3f803f80u : 0u;
    *(LAS u32x4*)(L + L_ONES + tid * 16) = (u32x4){ov, ov, ov, ov};
    if (kvwg) {
#pragma unroll
      for (int i = 0; i < 4; ++i) *(LAS u32x4*)(L + L_H1 + w * 4096 + i * 1024 + lane * 16) = wm[i];
      const LAS unsigned char* b0 = L + L_H1 + w * 4096 + (8 * g + (c >> 2)) * 32 + (c & 3) * 8;
      asm volatile("s_waitcnt lgkmcnt(0)" ::: "memory");
#pragma unroll
      for (int i = 0; i < 4; ++i) {
        const u32x2 lo = tr_read(b0 + i * 1024), hi2 = tr_read(b0 + i * 1024 + 128);
        kfr[i] = (u32x4){lo[0], lo[1], hi2[0], hi2[1]};
      }
    }
  }
  STAMP(23);
  u32x4 mg[4];
  CFENCE();
#pragma unroll
  for (int i = 0; i < 4; ++i) mg[i] = LD16(tid * 16, WS_MODG + (bid & 15) * 32768 + i * 8192);
  SBAR();
  if (late) MOD_PART(4);
  float xh[8][8];
#pragma unroll
  for (int i = 0; i < 8; ++i) {
#pragma unroll
    for (int q = 0; q < 4; ++q) { xh[i][2 * q] = fmaf(bflo(xr[i][q]), rsd[i], nb[i]); xh[i][2 * q + 1] = fmaf(bfhi(xr[i][q]), rsd[i], nb[i]); }
    asm volatile("" : "+v"(xh[i][0]), "+v"(xh[i][1]), "+v"(xh[i][2]), "+v"(xh[i][3]), "+v"(xh[i][4]), "+v"(xh[i][5]), "+v"(xh[i][6]), "+v"(xh[i][7]));
  }
  SBAR();
  STAMP(2);
  {
    const unsigned thi = (unsigned)(epoch >> 32), tlo = (unsigned)epoch;
#pragma unroll 1
    for (int spins = 0; spins < SPIN_CAP; ++spins) {
      unsigned bad = 0u;
#pragma unroll
      for (int i = 0; i < 4; ++i) bad |= (mg[i][1] ^ thi) | (mg[i][3] ^ thi) | (((mg[i][0] ^ tlo) | (mg[i][2] ^ tlo)) & 0xff000000u);
      if (__all(bad == 0u)) break;
      __builtin_amdgcn_s_sleep(2);
      CFENCE();
#pragma unroll
      for (int i = 0; i < 4; ++i) mg[i] = LD16(tid * 16, WS_MODG + (bid & 15) * 32768 + i * 8192);
    }
    const float a0 = (__uint_as_float(mg[0][0] << 8) + __uint_as_float(mg[1][0] << 8)) + (__uint_as_float(mg[2][0] << 8) + __uint_as_float(mg[3][0] << 8));
    const float a1 = (__uint_as_float(mg[0][2] << 8) + __uint_as_float(mg[1][2] << 8)) + (__uint_as_float(mg[2][2] << 8) + __uint_as_float(mg[3][2] << 8));
    unsigned t = pack2(a0, a1);
    if (w >= 4) t = pack2(1.f + bflo(t), 1.f + bfhi(t));
    *(LAS unsigned*)(L + L_MODA + tid * 4) = t;
  }
  __syncthreads();
  STAMP(3);
  u32x4 wf[16];
  {
    const u32x4 sa = *(const LAS u32x4*)(L + L_MODA + lane * 16), ca = *(const LAS u32x4*)(L + L_MODA + 1024 + lane * 16);
    float sc[8], sh[8];
#pragma unroll
    for (int q = 0; q < 4; ++q) { sc[2 * q] = bflo(ca[q]); sc[2 * q + 1] = bfhi(ca[q]); sh[2 * q] = bflo(sa[q]); sh[2 * q + 1] = bfhi(sa[q]); }
#define H_ROW(i) do { u32x4 o_; \
      _Pragma("unroll") for (int q = 0; q < 4; ++q) o_[q] = pack2(fmaf(xh[i][2 * q], sc[2 * q], sh[2 * q]), fmaf(xh[i][2 * q + 1], sc[2 * q + 1], sh[2 * q + 1])); \
      *(LAS u32x4*)(L + L_H1 + XROW(i) * HROW + lane * 16) = o_; } while (0)
    H_ROW(0); H_ROW(1);
    if (kvwg) {
      __syncthreads();
      const int sub = m - 4;
      const LAS unsigned char* hp = L + L_H1 + (16 * sub + c) * HROW + g * 16 + w * 128;
      const u32x4 h0 = *(const LAS u32x4*)hp, h1 = *(const LAS u32x4*)(hp + 64);
      f32x4 ak = {0.f, 0.f, 0.f, 0.f}, av = ak;
      ak = mfma16(kfr[0], h0, ak); av = mfma16(h0, kfr[2], av);
      ak = mfma16(kfr[1], h1, ak); av = mfma16(h1, kfr[3], av);
      *(LAS f32x4*)(L + L_WKV + w * 2048 + lane * 16) = ak;
      *(LAS f32x4*)(L + L_WKV + w * 2048 + 1024 + lane * 16) = av;
      __syncthreads();
      if (w < 2) {
        ak = *(const LAS f32x4*)(L + L_WKV + w * 1024 + lane * 16);
#pragma unroll
        for (int i = 1; i < 8; ++i) ak += *(const LAS f32x4*)(L + L_WKV + i * 2048 + w * 1024 + lane * 16);
        const u32x2 ok = {min(pack2(ak[0], ak[1]), SENT - 1u), min(pack2(ak[2], ak[3]), SENT - 1u)};
        const int tile = 2 * rb + (sub >> 1), s = sub & 1;
        const int kvo = WS_KV + (tile >> 3) * 16384 + (tile & 7) * 1024 + (w ? 8192 + s * 512 : s * 256);
        __builtin_amdgcn_raw_buffer_store_b64(ok, rs, w ? (g & 1) * 256 + c * 16 + (g >> 1) * 8 : ((g >> 1) * 32 + c) * 16 + (g & 1) * 8, kvo, AUX_SC1);
        STAMP(26);
      }
    } else if (late && w == 0) {
      __builtin_amdgcn_raw_buffer_store_b32(__float_as_uint(MOD_SUM()), rs, lane * 4, WS_MODP + (m & 1) * 8192 + (bid & 31) * 256, AUX_SC1);
    }
    const int q4 = c >> 2, p4 = c & 3;
    const LAS unsigned char* a0 = L + (8 * g + q4) * 128 + ((hh ^ (((q4 >> 1) & 1) | ((g & 1) << 1))) << 5) + p4 * 8;
#pragma unroll
    for (int ks = 0; ks < 16; ++ks) {
      const u32x2 lo = tr_read(a0 + ks * 4096), hi2 = tr_read(a0 + ks * 4096 + 512);
      wf[ks] = (u32x4){lo[0], lo[1], hi2[0], hi2[1]};
    }
    H_ROW(2); H_ROW(3); H_ROW(4); H_ROW(5); H_ROW(6); H_ROW(7);
  }
  __syncthreads();
  STAMP(4);
  const int kvw = (w >> 1) * 16384 + (w & 1) * 4096;
  u32x4 kvs[8];
  CFENCE();
#pragma unroll
  for (int i = 0; i < 8; ++i) kvs[i] = LD16(lane * 16, WS_KV + kvw + (i >> 2) * 8192 + (i & 3) * 1024);
  SBAR();
  u32x4 qf;
  {
    f32x4 acc0 = {0.f, 0.f, 0.f, 0.f}, acc1 = acc0;
    const LAS unsigned char* hp = L + L_H1 + (32 * rt + c) * HROW + g * 16;
    u32x4 b0[16], b1[16];
#pragma unroll
    for (int ks = 0; ks < 4; ++ks) { b0[ks] = *(const LAS u32x4*)(hp + ks * 64); b1[ks] = *(const LAS u32x4*)(hp + 16 * HROW + ks * 64); }
    SBAR();
#pragma unroll
    for (int ks = 0; ks < 16; ++ks) {
      if (ks + 4 < 16) { b0[ks + 4] = *(const LAS u32x4*)(hp + (ks + 4) * 64); b1[ks + 4] = *(const LAS u32x4*)(hp + 16 * HROW + (ks + 4) * 64); }
      acc0 = mfma16(wf[ks], b0[ks], acc0);
      acc1 = mfma16(wf[ks], b1[ks], acc1);
      SBAR();
    }
    auto r0 = __builtin_amdgcn_permlane16_swap(pack2(acc0[0] * QSCALE, acc0[1] * QSCALE), pack2(acc1[0] * QSCALE, acc1[1] * QSCALE), false, false);
    auto r1 = __builtin_amdgcn_permlane16_swap(pack2(acc0[2] * QSCALE, acc0[3] * QSCALE), pack2(acc1[2] * QSCALE, acc1[3] * QSCALE), false, false);
    qf = (u32x4){r0[0], r1[0], r0[1], r1[1]};
  }
  STAMP(5);

#define KV_COMMIT(HALF, SLOT) do { \
    unsigned mx = 0u; \
    _Pragma("unroll") for (int i = 0; i < 8; ++i) mx = max(mx, umax4(kvs[i])); \
    if (__builtin_expect(__any(mx == SENT), 0)) { \
      _Pragma("unroll 1") for (int spins = 0; spins < SPIN_CAP; ++spins) { \
        unsigned bad = 0u; \
        _Pragma("unroll") for (int i = 0; i < 8; ++i) bad |= __any(umax4(kvs[i]) == SENT) ? 1u << i : 0u; \
        if (!bad) break; \
        __builtin_amdgcn_s_sleep(1); \
        CFENCE(); \
        _Pragma("unroll") for (int i = 0; i < 8; ++i) \
          if (bad >> i & 1u) kvs[i] = LD16(lane * 16, WS_KV + (HALF) * 65536 + kvw + (i >> 2) * 8192 + (i & 3) * 1024); \
      } \
    } \
    _Pragma("unroll") for (int i = 0; i < 8; ++i) *(LAS u32x4*)(L + (HALF) * 65536 + kvw + (i >> 2) * 8192 + (i & 3) * 1024 + lane * 16) = kvs[i]; \
    float km = fmaxf(fold32(knorm2(kvs[0]), knorm2(kvs[1])), fold32(knorm2(kvs[2]), knorm2(kvs[3]))); \
    km = max32(max16(row16_max(km))); \
    if (lane == 0) *(LAS float*)(L + L_CNT + 32 + (SLOT) * 32 + w * 4) = km; } while (0)
#define KMAX8(SLOT) ({ const f32x4 k0 = *(const LAS f32x4*)(L + L_CNT + 32 + (SLOT) * 32), k1 = *(const LAS f32x4*)(L + L_CNT + 48 + (SLOT) * 32); \
    fmaxf(fmaxf(fmaxf(k0[0], k0[1]), fmaxf(k0[2], k0[3])), fmaxf(fmaxf(k1[0], k1[1]), fmaxf(k1[2], k1[3]))); })
  float q2;
  {
    q2 = knorm2(qf);
    q2 = fold32(q2, q2);
    STAMP(27);
    KV_COMMIT(0, 0);
    STAMP(29);
  }
  u16 ge[2][8];
  int gdst, gdd;
  {
    const int pi = bid * 8 + w;
    const u16* src; int ld, rbase, col, up, dcol, bytes;
    if (pi < 256) { const int mm = pi >> 5, ks = (pi >> 1) & 15, tp = pi & 1;
      src = wo; ld = 512; up = 8; dcol = 4; bytes = 512 * 512 * 2; gdst = WS_WO + pi * 2048; gdd = 1024;
      rbase = 16 * (2 * ks + (g >> 1)) + 4 * (g & 1); col = 64 * mm + 32 * tp + 8 * (c >> 2) + (c & 3); }
    else if (pi < 1152) { const int id = pi - 256, a = id >> 5, ks = id & 31;
      src = w1; ld = F_; up = 4; dcol = 32; bytes = 512 * F_ * 2; gdst = WS_W1 + (a * 64 + ks) * 1024; gdd = 32768;
      rbase = 16 * ks + 8 * hi; col = 64 * a + c32; }
    else { const int id = pi - 1152, mm = id / 112, ks = id - mm * 112;
      src = w2; ld = 512; up = 8; dcol = 32; bytes = 512 * F_ * 2; gdst = WS_W2 + id * 2048; gdd = 1024;
      rbase = 16 * ks + 4 * hi; col = 64 * mm + 16 * ((c32 >> 2) & 1) + (c32 & 3) + 4 * (c32 >> 3); }
    const __amdgpu_buffer_rsrc_t gs = __builtin_amdgcn_make_buffer_rsrc((void*)src, 0, bytes, 0x00020000);
    const int vo = (rbase * ld + col) * 2;
#pragma unroll
    for (int f = 0; f < 2; ++f)
#pragma unroll
      for (int j = 0; j < 4; ++j) {
        ge[f][j] = __builtin_amdgcn_raw_buffer_load_b16(gs, vo, (j * ld + f * dcol) * 2, 0);
        ge[f][4 + j] = __builtin_amdgcn_raw_buffer_load_b16(gs, vo, ((up + j) * ld + f * dcol) * 2, 0);
      }
  }
  SBAR();
  __syncthreads();
  f32x16 o, cin;
  const float kmA = KMAX8(0);
  const float boundA = __builtin_amdgcn_sqrtf(q2 * kmA) * 1.0001f;
#pragma unroll
  for (int r = 0; r < 16; ++r) { cin[r] = -boundA; o[r] = 0.f; }
  STAMP(6);
  unsigned kp = lds0 + lane * 16;
  unsigned vp = lds0 + ((c32 < 16) ? (8192 + hi * 256 + c32 * 16) : (L_ONES + (c32 - 16) * 16));
  const unsigned vstep = (c32 < 16) ? 16384 : 0;
  f32x16 sA = mfma32(*(const LAS u32x4*)(L + lane * 16), qf, cin);
  u32x4 kf = *(const LAS u32x4*)(L + lane * 16 + 1024);
  u32x4 v0 = *(const LAS u32x4*)(L + (vp - lds0));
  u32x4 v1 = {0u, 0u, 0u, 0u}, p1 = {0u, 0u, 0u, 0u};
  asm volatile("s_nop 7\n\ts_nop 3" : "+{v[192:207]}"(sA));
  const unsigned l16 = lane * 16, l16b = lane * 16 + 8192;
  __builtin_amdgcn_s_setprio(PRIO0);
  {
    u32x16 ka, kb;
    SBAR();
    asm volatile(ATT_ASM(GL("v[32:35]", "%[va]", "0"), "", GL("v[36:39]", "%[va]", "1024"), "", GL("v[40:43]", "%[va]", "2048"), "",
                         GL("v[44:47]", "%[va]", "3072"), "", GL("v[48:51]", "%[vb]", "0"), "", GL("v[52:55]", "%[vb]", "1024"), "",
                         GL("v[56:59]", "%[vb]", "2048"), GL("v[60:63]", "%[vb]", "3072"), "", "") "s_waitcnt vmcnt(0)\n"
                 : ATT_STATE, "=&{v[32:47]}"(ka), "=&{v[48:63]}"(kb)
                 : ATT_IN, [va] "v"(l16), [vb] "v"(l16b), [sb] "s"(ws + WS_KV + 65536 + kvw) : ATT_CLOB);
    SBAR();
    u32x4 kvs[8] = {SUB4(ka, 0), SUB4(ka, 1), SUB4(ka, 2), SUB4(ka, 3), SUB4(kb, 0), SUB4(kb, 1), SUB4(kb, 2), SUB4(kb, 3)};
    KV_COMMIT(1, 1);
    lds_arrive(L + L_CNT, lane);
#pragma unroll
    for (int f = 0; f < 2; ++f) {
      u32x4 t;
#pragma unroll
      for (int q = 0; q < 4; ++q) t[q] = (unsigned)ge[f][2 * q] | ((unsigned)ge[f][2 * q + 1] << 16);
      ST16(t, lane * 16, gdst + f * gdd);
    }
  }
  STAMP(7);
  __builtin_amdgcn_s_setprio(PRIO1);
  const int nt = 7 * m + (w < 7 ? w : 0);
  {
#define GS(B, OFF) "global_store_dwordx4 %[va], %[sent], " B " offset:" OFF " sc1\n"
    const u32x4 sent = {SENT, SENT, SENT, SENT};
    unsigned char* h0 = w < 7 ? ws + WS_HB + (rb * 224 + 2 * nt) * 1024 : ws + WS_ST + rb * 8192 + m * 1024 - 1024;
    SBAR();
    asm volatile(ATT_ASM(GS("%[so]", "0"), GS("%[sy]", "0"), GS("%[s0]", "1024"), GS("%[s1]", "0"), GS("%[s2]", "0"), GS("%[s2]", "1024"),
                         "", "", "", "", "", "", "", "", "", "")
                 : ATT_STATE
                 : ATT_IN, [va] "v"(l16), [sent] "v"(sent), [so] "s"(ws + WS_OB + ((rb * 2 + rt) * 32 + 4 * m + hh) * 1024),
                   [sy] "s"(ws + WS_YB + ((rb * 2 + (w >> 2)) * 32 + 4 * m + (w & 3)) * 1024), [s0] "s"(h0),
                   [s1] "s"(ws + WS_HB + (rb * 224 + 2 * nt) * 1024), [s2] "s"(ws + WS_HB + (rb * 224 + 112 + 2 * nt) * 1024) : ATT_CLOB);
    SBAR();
  }
  DRAIN();
  if (lds_arrive(L + L_CNT + 4, lane) == 7u && lane == 0) __hip_atomic_store((GAS unsigned*)(ws + WS_AUXF + bid * 4), 1u, RLX_AGENT);
  __builtin_amdgcn_s_setprio(PRIO2);
  ATT_PLAIN();
  lds_wait8(L + L_CNT);
  {
    const float boundB = __builtin_amdgcn_sqrtf(q2 * fmaxf(kmA, KMAX8(1))) * 1.0001f;
    const float dl = boundA - boundB, f = __builtin_amdgcn_exp2f(dl);
    o = mfma32(v1, p1, o);
    p1 = (u32x4){0u, 0u, 0u, 0u};
#pragma unroll
    for (int r = 0; r < 16; ++r) { sA[r] += dl; cin[r] = -boundB; }
#pragma unroll
    for (int r = 0; r < 9; ++r) o[r] *= f;
  }
  __builtin_amdgcn_s_setprio(PRIO3);
  ATT_PLAIN();
  lds_arrive(L + L_CNT + 8, lane);
  CFENCE();
  u32x4 af = LD16(lane * 16, WS_AUXF);
  __builtin_amdgcn_s_setprio(PRIO4);
  ATT_PLAIN();
#pragma unroll 1
  for (int spins = 0; spins < SPIN_CAP && !__all((af[0] & af[1] & af[2] & af[3]) == 1u && (af[0] | af[1] | af[2] | af[3]) == 1u); ++spins) {
    __builtin_amdgcn_s_sleep(2);
    CFENCE();
    af = LD16(lane * 16, WS_AUXF);
  }
  if (bid == 0 && tid == 0) __hip_atomic_store((GAS u64*)(ws + WS_EPOCH), epoch, RLX_AGENT);
  STAMP(8);
  const f32x4 tb0 = LDF4(tid * 16, WS_MODP), tb1 = LDF4(tid * 16, WS_MODP + 8192);
  u32x16 w1t[8];
#define ATT_W1(B, T0, T1, T2, T3) do { SBAR(); \
    asm volatile(ATT_ASM(GL("v[" #B "+0:" #B "+3]", "%[va]", "-4096"), GL("v[" #B "+4:" #B "+7]", "%[va]", "-3072"), \
                         GL("v[" #B "+8:" #B "+11]", "%[va]", "-2048"), GL("v[" #B "+12:" #B "+15]", "%[va]", "-1024"), \
                         GL("v[" #B "+16:" #B "+19]", "%[va]", "0"), GL("v[" #B "+20:" #B "+23]", "%[va]", "1024"), \
                         GL("v[" #B "+24:" #B "+27]", "%[va]", "2048"), GL("v[" #B "+28:" #B "+31]", "%[va]", "3072"), \
                         GL("v[" #B "+32:" #B "+35]", "%[vb]", "-4096"), GL("v[" #B "+36:" #B "+39]", "%[vb]", "-3072"), \
                         GL("v[" #B "+40:" #B "+43]", "%[vb]", "-2048"), GL("v[" #B "+44:" #B "+47]", "%[vb]", "-1024"), \
                         GL("v[" #B "+48:" #B "+51]", "%[vb]", "0"), GL("v[" #B "+52:" #B "+55]", "%[vb]", "1024"), \
                         GL("v[" #B "+56:" #B "+59]", "%[vb]", "2048"), GL("v[" #B "+60:" #B "+63]", "%[vb]", "3072")) \
                 : ATT_STATE, T0, T1, T2, T3 \
                 : ATT_IN, [va] "v"(l16), [vb] "v"(l16b), [sb] "s"(ws + WS_W1 + nt * 32768 + ((B) - 32) * 256 + 4096) : ATT_CLOB); SBAR(); } while (0)
  __builtin_amdgcn_s_setprio(PRIO5);
  ATT_W1(32, "=&{v[32:47]}"(w1t[0]), "=&{v[48:63]}"(w1t[1]), "=&{v[64:79]}"(w1t[2]), "=&{v[80:95]}"(w1t[3]));
  __builtin_amdgcn_s_setprio(PRIO6);
  ATT_W1(96, "=&{v[96:111]}"(w1t[4]), "=&{v[112:127]}"(w1t[5]), "=&{v[128:143]}"(w1t[6]), "=&{v[144:159]}"(w1t[7]));
  lds_wait8(L + L_CNT + 8);
  __builtin_amdgcn_s_setprio(PRIO7);
  {
    unsigned sv;
    SBAR();
    asm volatile("s_mov_b32 %[sv], m0\ns_mov_b32 m0, %[ld]\n"
                 ATT_ASM(DMA("%[va]", "0"), DMA("%[va]", "1024"), DMA("%[va]", "2048"), DMA("%[va]", "3072") "s_add_u32 m0, m0, 0x2000\n",
                         DMA("%[vb]", "0"), DMA("%[vb]", "1024"), DMA("%[vb]", "2048"), DMA("%[vb]", "3072"), "", "", "", "", "", "", "", "")
                 "s_mov_b32 m0, %[sv]\n"
                 : ATT_STATE, [sv] "=&s"(sv)
                 : ATT_IN, [va] "v"(l16), [vb] "v"(l16b), [sb] "s"(ws + WS_WO + m * 65536 + kvw), [ld] "s"(lds0 + kvw) : ATT_CLOB, "scc");
    SBAR();
  }
  asm volatile("s_waitcnt vmcnt(0)" : "+v"(w1t[0]), "+v"(w1t[1]), "+v"(w1t[2]), "+v"(w1t[3]), "+v"(w1t[4]), "+v"(w1t[5]), "+v"(w1t[6]), "+v"(w1t[7]) :: "memory");
  o = mfma32(v1, p1, o);
  {
  const int lane = fresh_lane(), tid = w * 64 + lane;
  const int g = lane >> 4, c = lane & 15, hi = lane >> 5, c32 = lane & 31;
  {
    const f32x4 t = tb0 + tb1;
    u32x2 tp = {pack2(t[0], t[1]), pack2(t[2], t[3])};
    if ((w >> 1) == 2) tp = (u32x2){pack2(1.f + bflo(tp[0]), 1.f + bfhi(tp[0])), pack2(1.f + bflo(tp[1]), 1.f + bfhi(tp[1]))};
    *(LAS u32x2*)(L + L_TAB + tid * 8) = tp;
  }
  if (!__all(o[8] >= 5.421010862427522e-20f)) {
    f32x16 z;
#pragma unroll
    for (int r = 0; r < 16; ++r) { z[r] = 0.f; o[r] = 0.f; }
    float mrun = -INFINITY;
    const unsigned ov = (c32 >= 16 && (c & 11) == 0) ? 0x3f803f80u : 0u;
#pragma unroll 1
    for (int t = 0; t < 64; ++t) {
      const int tb = WS_KV + (t >> 3) * 16384 + (t & 7) * 1024;
      const u32x4 kk = LD16(lane * 16, tb);
      u32x4 va = LD16(hi * 256 + c * 16, tb + 8192);
      u32x4 vb = LD16(hi * 256 + c * 16, tb + 8192 + 512);
      if (c32 >= 16) { va = (u32x4){ov, ov, ov, ov}; vb = va; }
      const f32x16 s = mfma32(kk, qf, z);
      float tm = s[0];
#pragma unroll
      for (int r = 1; r < 16; ++r) tm = fmaxf(tm, s[r]);
      const float mn = fmaxf(mrun, max32(tm));
      const float al = __builtin_amdgcn_exp2f(mrun - mn);
      mrun = mn;
      u32x4 q0, q1;
#pragma unroll
      for (int q = 0; q < 4; ++q) {
        q0[q] = pack2(__builtin_amdgcn_exp2f(s[2 * q] - mn), __builtin_amdgcn_exp2f(s[2 * q + 1] - mn));
        q1[q] = pack2(__builtin_amdgcn_exp2f(s[8 + 2 * q] - mn), __builtin_amdgcn_exp2f(s[8 + 2 * q + 1] - mn));
      }
#pragma unroll
      for (int r = 0; r < 16; ++r) o[r] *= al;
      o = mfma32(va, q0, o);
      o = mfma32(vb, q1, o);
    }
  }
  STAMP(9);
#define CANON(v) do { _Pragma("unroll") for (int q_ = 0; q_ < 4; ++q_) v[q_] = min(v[q_], SENT - 1u); } while (0)
  {
    const float rl = __builtin_amdgcn_rcpf(o[8]);
    u32x4 of;
#pragma unroll
    for (int q = 0; q < 4; ++q) of[q] = pack2(o[2 * q] * rl, o[2 * q + 1] * rl);
    CANON(of);
    ST16(of, lane * 16, WS_OB + ((rb * 2 + rt) * 32 + 4 * m + hh) * 1024);
  }
  publish(ws, WS_FLAG + (0 * 256 + rb * 8 + m) * 128, tid);
  STAMP(10);
#define W1FRAG(ks) (((ks) & 3) == 0 ? SUB4(w1t[(ks) >> 2], 0) : ((ks) & 3) == 1 ? SUB4(w1t[(ks) >> 2], 1) : ((ks) & 3) == 2 ? SUB4(w1t[(ks) >> 2], 2) : SUB4(w1t[(ks) >> 2], 3))
#define FETCH8(DST, VOFF, SOFF, EXTRA_LD, EXTRA_MAX, KIND, FIRST, N) do { \
    _Pragma("unroll 1") for (int pass = 0; pass < 2; ++pass) { \
      CFENCE(); \
      _Pragma("unroll") for (int i = 0; i < 8; ++i) DST[i] = LD16(VOFF, SOFF); \
      EXTRA_LD; \
      if (pass == 0) { UV_PART; } \
      unsigned mx = EXTRA_MAX; \
      _Pragma("unroll") for (int i = 0; i < 8; ++i) mx = max(mx, umax4(DST[i])); \
      if (__builtin_expect(!__any(mx == SENT), 1)) break; \
      wait_flags(ws, KIND, FIRST, N, lane); \
    } } while (0)

  const int rs4 = w & 3, kh = w >> 2;
  const int erow = 64 * rb + 16 * rs4 + c, ecol = 64 * m + 32 * kh + 8 * g;
  f32x16 uv;
#pragma unroll
  for (int r = 0; r < 16; ++r) uv[r] = 0.f;
  const LAS unsigned char* tp = L + L_TAB + 2048 - (c32 & 1) * 1024 + hi * 16;
  u32x4 x1p;
  {
    const u32x4 xin = ldg16(x + (size_t)erow * D_ + ecol);
    u32x4 bf[8];
#define UV_PART _Pragma("unroll") for (int ks = 0; ks < 16; ++ks) uv = mfma32(W1FRAG(ks), *(const LAS u32x4*)(tp + ks * 32), uv)
    FETCH8(bf, g * 512 + c * 16, WS_OB + ((rb * 2 + (rs4 >> 1)) * 32 + 2 * (8 * kh + i)) * 1024 + (rs4 & 1) * 256, , 0u, 0, rb * 8 + 4 * kh, 4);
#undef UV_PART
    STAMP(11);
    f32x4 acc[4];
#pragma unroll
    for (int t = 0; t < 4; ++t) acc[t] = (f32x4){0.f, 0.f, 0.f, 0.f};
    const LAS unsigned char* wp = L + kh * 32768 + lane * 16;
    u32x4 wq4[32];
#pragma unroll
    for (int n = 0; n < 8; ++n) wq4[n] = *(const LAS u32x4*)(wp + n * 1024);
    SBAR();
#pragma unroll
    for (int n = 0; n < 32; ++n) {
      if (n + 8 < 32) wq4[n + 8] = *(const LAS u32x4*)(wp + (n + 8) * 1024);
      acc[n & 3] = mfma16(wq4[n], bf[n >> 2], acc[n & 3]);
      SBAR();
    }
    LAS f32x4* ex = (LAS f32x4*)(L + 65536);
    ex[(w * 2 + 0) * 64 + lane] = kh ? acc[0] : acc[2];
    ex[(w * 2 + 1) * 64 + lane] = kh ? acc[1] : acc[3];
    const u32x4 ga = *(const LAS u32x4*)(L + L_TAB + ecol * 2), cb = *(const LAS u32x4*)(L + L_TAB + 2048 + ecol * 2);
    __syncthreads();
    const f32x4 r0 = ex[((w ^ 4) * 2 + 0) * 64 + lane], r1 = ex[((w ^ 4) * 2 + 1) * 64 + lane];
    const f32x4 a0 = (kh ? acc[2] : acc[0]) + r0, a1 = (kh ? acc[3] : acc[1]) + r1;
    x1p[0] = pack2(fmaf(bflo(ga[0]), a0[0], bflo(xin[0])), fmaf(bfhi(ga[0]), a0[1], bfhi(xin[0])));
    x1p[1] = pack2(fmaf(bflo(ga[1]), a0[2], bflo(xin[1])), fmaf(bfhi(ga[1]), a0[3], bfhi(xin[1])));
    x1p[2] = pack2(fmaf(bflo(ga[2]), a1[0], bflo(xin[2])), fmaf(bfhi(ga[2]), a1[1], bfhi(xin[2])));
    x1p[3] = pack2(fmaf(bflo(ga[3]), a1[2], bflo(xin[3])), fmaf(bfhi(ga[3]), a1[3], bfhi(xin[3])));
    u32x4 y;
#pragma unroll
    for (int q = 0; q < 4; ++q) y[q] = pack2(bflo(x1p[q]) * bflo(cb[q]), bfhi(x1p[q]) * bfhi(cb[q]));
    CANON(y);
    ST16(y, g * 512 + c * 16, WS_YB + ((rb * 2 + (rs4 >> 1)) * 32 + 4 * m + 2 * kh) * 1024 + (rs4 & 1) * 256);
    float st = fold32(dot2bf(x1p[0], 0x3f803f80u, dot2bf(x1p[1], 0x3f803f80u, dot2bf(x1p[2], 0x3f803f80u, dot2bf(x1p[3], 0x3f803f80u, 0.f)))), knorm2(x1p));
    st = fold16(st, st);
    if ((g & 1) == 0)
      __builtin_amdgcn_raw_buffer_store_b32(min(__float_as_uint(st), SENT - 1u), rs, c * 8 + hi * 4, WS_ST + rb * 8192 + (2 * m + kh) * 512 + rs4 * 128, AUX_SC1);
  }
  PIN(x1p);
  STAMP(12);
  publish(ws, WS_FLAG + (1 * 256 + rb * 8 + m) * 128, tid);
  STAMP(13);

  constexpr int NW2 = 21;
  u32x4 w2f[28];
  const int w2o = WS_W2 + ((m * 112 + 14 * w) * 2) * 1024;
  {
    u32x4 yr[8], sp;
#define UV_PART _Pragma("unroll") for (int ks = 16; ks < 32; ++ks) uv = mfma32(W1FRAG(ks), *(const LAS u32x4*)(tp + ks * 32), uv)
    FETCH8(yr, lane * 16, WS_YB + rb * 65536 + (w * 8 + i) * 1024, sp = LD16((lane >> 2) * 512 + (lane & 3) * 16, WS_ST + rb * 8192 + w * 64), umax4(sp), 1, rb * 8, 8);
#undef UV_PART
    STAMP(14);
#pragma unroll
    for (int i = 0; i < 8; ++i) *(LAS u32x4*)(L + (w * 8 + i) * 1024 + lane * 16) = yr[i];
    float s1 = fold32(__uint_as_float(sp[0]), __uint_as_float(sp[2])), s2 = fold32(__uint_as_float(sp[1]), __uint_as_float(sp[3]));
    s1 = fold16(s1, s1); s2 = fold16(s2, s2);
    s1 += dpp<0x128>(s1); s2 += dpp<0x128>(s2);
    s1 += dpp<0x124>(s1); s2 += dpp<0x124>(s2);
    const float mu = s1 * (1.f / D_);
    const float rstd = __builtin_amdgcn_rsqf(fmaxf(fmaf(-mu, mu, s2 * (1.f / D_)), 0.f) + LN_EPS);
    if (c32 < 4) *(LAS f32x2*)(L + L_STAT + (8 * w + 2 * c32 + hi) * 8) = (f32x2){rstd, -mu};
    __syncthreads();
    STAMP(15);
    if (w < 7) {
      const LAS unsigned char* hp = L + lane * 16;
      const f32x2 st0 = *(const LAS f32x2*)(L + L_STAT + c32 * 8), st1 = *(const LAS f32x2*)(L + L_STAT + 256 + c32 * 8);
      f32x16 acc0, acc1;
#pragma unroll
      for (int r = 0; r < 16; ++r) { acc0[r] = 0.f; acc1[r] = 0.f; }
      u32x4 bq[64];
      u32x4 h0, h1;
      const int ho = WS_HB + (rb * 224 + 2 * nt) * 1024;
#define PRE(A, r, ST) fmaf(fmaf(dpp<0xA0>(uv[r]), ST[1], A[r]), ST[0], dpp<0xF5>(uv[r]))
#define HPK(A, q, ST) ({ const float a_ = PRE(A, 2 * (q), ST), b_ = PRE(A, 2 * (q) + 1, ST); min(pack2(SILU(a_), SILU(b_)), SENT - 1u); })
#pragma unroll
      for (int n = 0; n < 4; ++n) bq[n] = *(const LAS u32x4*)(hp + n * 1024);
      SBAR();
#pragma unroll
      for (int n = 0; n < 64; ++n) {
        if (n + 4 < 64) bq[n + 4] = *(const LAS u32x4*)(hp + (n + 4) * 1024);
        if (n < 32) acc0 = mfma32(W1FRAG(n & 31), bq[n], acc0); else acc1 = mfma32(W1FRAG(n & 31), bq[n], acc1);
        if ((n & 3) == 0 && 10 + n / 4 < NW2) w2f[10 + n / 4] = LD16(lane * 16, w2o + (10 + n / 4) * 1024);
        if (n >= 34 && n < 58 && (n - 34) % 3 == 0) {
          const int q = (n - 34) / 3;
          const unsigned t = HPK(acc0, q, st0);
          if (q < 4) { h0[q] = t; PIN(h0); } else { h1[q - 4] = t; PIN(h1); }
        }
        if (n == 58) { ST16(h0, lane * 16, ho); ST16(h1, lane * 16, ho + 1024); }
        SBAR();
      }
#pragma unroll
      for (int q = 0; q < 4; ++q) h0[q] = HPK(acc1, q, st1);
      ST16(h0, lane * 16, ho + 112 * 1024);
#pragma unroll
      for (int q = 0; q < 4; ++q) h1[q] = HPK(acc1, 4 + q, st1);
      ST16(h1, lane * 16, ho + 113 * 1024);
    } else {
#pragma unroll 1
      for (int v = 0; v < 8; ++v) {
        const unsigned char* gp = ws + WS_W2 + (m * 112 + 14 * v) * 2048;
        unsigned sv;
        asm volatile("s_mov_b32 %[sv], m0\ns_mov_b32 m0, %[ld]\ns_nop 0\n"
                     DMA("%[va]", "0") DMA("%[va]", "1024") DMA("%[va]", "2048") DMA("%[va]", "3072") "s_add_u32 m0, m0, 0x1000\ns_nop 0\n"
                     DMA("%[vb]", "0") DMA("%[vb]", "1024") DMA("%[vb]", "2048") DMA("%[vb]", "3072") "s_add_u32 m0, m0, 0x1000\ns_nop 0\n"
                     DMA("%[vc]", "0") DMA("%[vc]", "1024") "s_mov_b32 m0, %[sv]\n"
                     : [sv] "=&s"(sv)
                     : [va] "v"(lane * 16), [vb] "v"(lane * 16 + 4096), [vc] "v"(lane * 16 + 8192), [sb] "s"(gp), [ld] "s"(lds0 + L_W2 + v * 10240)
                     : "memory", "scc");
      }
#pragma unroll
      for (int i = 10; i < NW2; ++i) w2f[i] = LD16(lane * 16, w2o + i * 1024);
    }
  }
  STAMP(16);
#pragma unroll
  for (int i = NW2; i < 28; ++i) w2f[i] = LD16(lane * 16, w2o + i * 1024);
  publish(ws, WS_FLAG + (2 * 256 + rb * 8 + m) * 128, tid);
  STAMP(17);

  {
    if (w < 4) __builtin_amdgcn_s_setprio(1);
    const int hb = WS_HB + (rb * 224 + 14 * w) * 1024;
    constexpr int WIN = 14;
    LAS unsigned char* rp = L + w * 16384 + c32 * 256;
    u32x4 hf[WIN];
    CFENCE();
#pragma unroll
    for (int n = 0; n < WIN; ++n) hf[n] = LD16(lane * 16, hb + n * 1024);
#pragma unroll
    for (int i = 0; i < 10; ++i) w2f[i] = *(const LAS u32x4*)(L + L_W2 + (w * 10 + i) * 1024 + lane * 16);
    lds_arrive(L + L_CNT + 12, lane);
#pragma unroll 1
    for (int pass = 0; pass < 2; ++pass) {
      f32x16 acc[2][2];
#pragma unroll
      for (int r = 0; r < 16; ++r) { acc[0][0][r] = 0.f; acc[0][1][r] = 0.f; acc[1][0][r] = 0.f; acc[1][1][r] = 0.f; }
      unsigned mx = 0u;
      SBAR();
#define RED_WR(R2, ct, q) *(LAS f32x4*)(rp + (R2) * 8192 + (((8 * (ct) + 4 * hi + (q)) ^ (c32 & 15)) << 4)) = \
        (f32x4){acc[R2][ct][4 * (q)], acc[R2][ct][4 * (q) + 1], acc[R2][ct][4 * (q) + 2], acc[R2][ct][4 * (q) + 3]}
#pragma unroll
      for (int n = 0; n < 28; ++n) {
        const u32x4 cur = hf[n % WIN];
        acc[n / 14][0] = mfma32(w2f[2 * (n % 14)], cur, acc[n / 14][0]);
        acc[n / 14][1] = mfma32(w2f[2 * (n % 14) + 1], cur, acc[n / 14][1]);
        mx = max(mx, umax4(cur));
        if (n < 14) hf[n] = LD16(lane * 16, hb + (112 + n) * 1024);
        if (n == 15) lds_wait8(L + L_CNT + 12);
        if (n >= 16 && n < 24) RED_WR(0, (n - 16) >> 2, (n - 16) & 3);
        SBAR();
      }
#pragma unroll
      for (int i = 0; i < 8; ++i) RED_WR(1, i >> 2, i & 3);
      if (__builtin_expect(!__any(mx == SENT), 1)) break;
      wait_flags(ws, 2, rb * 8 + w, 1, lane);
#pragma unroll
      for (int n = 0; n < WIN; ++n) hf[n] = LD16(lane * 16, hb + n * 1024);
    }
    STAMP(19);
    const int lane = fresh_lane(), g = lane >> 4, c = lane & 15;
    const int erow = 64 * rb + 16 * rs4 + c, ecol = 64 * m + 32 * kh + 8 * g;
    const u32x4 gb = *(const LAS u32x4*)(L + L_TAB + 3072 + ecol * 2);
    __syncthreads();
    f32x4 a0 = {0.f, 0.f, 0.f, 0.f}, a1 = a0;
#pragma unroll
    for (int p = 0; p < 8; ++p) {
      a0 += *(const LAS f32x4*)(L + p * 16384 + (16 * rs4 + c) * 256 + (((8 * kh + 2 * g) ^ c) << 4));
      a1 += *(const LAS f32x4*)(L + p * 16384 + (16 * rs4 + c) * 256 + (((8 * kh + 2 * g + 1) ^ c) << 4));
    }
    u32x4 xo;
    xo[0] = pack2(fmaf(bflo(gb[0]), a0[0], bflo(x1p[0])), fmaf(bfhi(gb[0]), a0[1], bfhi(x1p[0])));
    xo[1] = pack2(fmaf(bflo(gb[1]), a0[2], bflo(x1p[1])), fmaf(bfhi(gb[1]), a0[3], bfhi(x1p[1])));
    xo[2] = pack2(fmaf(bflo(gb[2]), a1[0], bflo(x1p[2])), fmaf(bfhi(gb[2]), a1[1], bfhi(x1p[2])));
    xo[3] = pack2(fmaf(bflo(gb[3]), a1[2], bflo(x1p[3])), fmaf(bfhi(gb[3]), a1[3], bfhi(x1p[3])));
    const __amdgpu_buffer_rsrc_t os = __builtin_amdgcn_make_buffer_rsrc(out, 0, 2048 * 512 * 2, 0x00020000);
    __builtin_amdgcn_raw_buffer_store_b128(xo, os, (erow * D_ + ecol) * 2, 0, AUX_SC1);
  }
  STAMP(20);
  }
}

extern "C" void kernel_launch(void* const* d_in, const int* in_sizes, int n_in, void* d_out, int out_size, void* d_ws, size_t ws_size,
                              hipStream_t stream) {
  fused_block<<<256, 512, 0, stream>>>((const u16*)d_in[0], (const u16*)d_in[1], (const u16*)d_in[2], (const u16*)d_in[3],
                                       (const u16*)d_in[4], (const u16*)d_in[6], (const u16*)d_in[7], (const u16*)d_in[8],
                                       (const u16*)d_in[9], (unsigned char*)d_ws, (u16*)d_out);
}
```

```cpp
#include <hip/hip_runtime.h>
#include <hip/hip_bf16.h>
#include <stdint.h>

typedef __bf16 bf16x8 __attribute__((ext_vector_type(8)));
typedef __bf16 bf16x2 __attribute__((ext_vector_type(2)));
typedef float f32x2 __attribute__((ext_vector_type(2)));
typedef float f32x4 __attribute__((ext_vector_type(4)));
typedef float f32x16 __attribute__((ext_vector_type(16)));
typedef unsigned u32x4 __attribute__((ext_vector_type(4)));
typedef unsigned u32x2 __attribute__((ext_vector_type(2)));
typedef unsigned u32x16 __attribute__((ext_vector_type(16)));
typedef short s16x4 __attribute__((ext_vector_type(4)));
typedef unsigned short u16;
typedef unsigned long long u64;
#define LAS __attribute__((address_space(3)))
#define GAS __attribute__((address_space(1)))
#define RLX_AGENT __ATOMIC_RELAXED, __HIP_MEMORY_SCOPE_AGENT
#define SBAR() __builtin_amdgcn_sched_barrier(0)
#define PIN(x) asm volatile("" : "+v"(x))
#define DRAIN() asm volatile("s_waitcnt vmcnt(0)" ::: "memory")
#define CFENCE() asm volatile("" ::: "memory")

constexpr int D_ = 512, F_ = 1792;
constexpr float QSCALE = 0.25f * 1.4426950408889634f;
constexpr float LN_EPS = 1e-5f;
constexpr int AUX_SC1 = 16;
constexpr unsigned SENT = 0xffffffffu;
constexpr int SPIN_CAP = 1 << 17;

constexpr int WS_EPOCH = 0;
constexpr int WS_KF = 163840;
constexpr int WS_AUXF = 180224;
constexpr int WS_FLAG = 16384;
constexpr int WS_MODP = 131072;
constexpr int WS_KV = 262144;
constexpr int WS_MODG = 393216;
constexpr int WS_OB = 1310720;
constexpr int WS_ST = 917504;
constexpr int WS_YB = WS_OB + 2097152;
constexpr int WS_HB = WS_YB + 2097152;
constexpr int WS_WO = WS_HB + 7340032;
constexpr int WS_W1 = WS_WO + 512 * 1024;
constexpr int WS_W2 = WS_W1 + 1792 * 1024;
constexpr int WS_END = WS_W2 + 1792 * 1024;
#define STAMP(i)
#define COUNT(k)
#define STAMPW(i)

constexpr int HROW = 1040;
constexpr int L_H1 = 65536;
constexpr int L_ONES = 132096;
constexpr int L_WKV = 140288;
constexpr int L_MODA = 156672;
constexpr int L_RED = 158720;
constexpr int L_CNT = 160768;
constexpr int L_END = 160896;
constexpr int L_W2 = 65536;
constexpr int L_TAB = L_MODA;
constexpr int L_STAT = 155648;
#define KV0 2
#define WRD 6
#define PRIO0 3
#define PRIO1 2
#define PRIO2 1
#define PRIO3 0
#define PRIO4 3
#define PRIO5 3
#define PRIO6 2
#define PRIO7 1

__device__ __forceinline__ unsigned pack2(float a, float b) {
  f32x2 v = {a, b};
  return __builtin_bit_cast(unsigned, __builtin_convertvector(v, bf16x2));
}
__device__ __forceinline__ float bflo(unsigned u) { return __uint_as_float(u << 16); }
__device__ __forceinline__ float bfhi(unsigned u) { return __uint_as_float(u & 0xffff0000u); }
__device__ __forceinline__ float bfr(float a) { return bflo(pack2(a, 0.f)); }
__device__ __forceinline__ f32x16 mfma32(u32x4 a, u32x4 b, f32x16 c) {
  return __builtin_amdgcn_mfma_f32_32x32x16_bf16(__builtin_bit_cast(bf16x8, a), __builtin_bit_cast(bf16x8, b), c, 0, 0, 0);
}
__device__ __forceinline__ f32x4 mfma16(u32x4 a, u32x4 b, f32x4 c) {
  return __builtin_amdgcn_mfma_f32_16x16x32_bf16(__builtin_bit_cast(bf16x8, a), __builtin_bit_cast(bf16x8, b), c, 0, 0, 0);
}
__device__ __forceinline__ float dot2bf(unsigned a, unsigned b, float c) {
  return __builtin_amdgcn_fdot2_f32_bf16(__builtin_bit_cast(bf16x2, a), __builtin_bit_cast(bf16x2, b), c, false);
}
template <int CTRL> __device__ __forceinline__ float dpp(float v) {
  return __int_as_float(__builtin_amdgcn_update_dpp(0, __float_as_int(v), CTRL, 0xf, 0xf, true));
}
__device__ __forceinline__ float row16_sum(float t) { t += dpp<0xB1>(t); t += dpp<0x4E>(t); t += dpp<0x141>(t); t += dpp<0x140>(t); return t; }
__device__ __forceinline__ float row16_max(float t) { t = fmaxf(t, dpp<0xB1>(t)); t = fmaxf(t, dpp<0x4E>(t)); t = fmaxf(t, dpp<0x141>(t)); t = fmaxf(t, dpp<0x140>(t)); return t; }
__device__ __forceinline__ float fold32(float a, float b) {
  auto r = __builtin_amdgcn_permlane32_swap(__float_as_uint(a), __float_as_uint(b), false, false);
  return __uint_as_float(r[0]) + __uint_as_float(r[1]);
}
__device__ __forceinline__ float fold16(float a, float b) {
  auto r = __builtin_amdgcn_permlane16_swap(__float_as_uint(a), __float_as_uint(b), false, false);
  return __uint_as_float(r[0]) + __uint_as_float(r[1]);
}
__device__ __forceinline__ float max32(float a) {
  auto r = __builtin_amdgcn_permlane32_swap(__float_as_uint(a), __float_as_uint(a), false, false);
  return fmaxf(__uint_as_float(r[0]), __uint_as_float(r[1]));
}
__device__ __forceinline__ float max16(float a) {
  auto r = __builtin_amdgcn_permlane16_swap(__float_as_uint(a), __float_as_uint(a), false, false);
  return fmaxf(__uint_as_float(r[0]), __uint_as_float(r[1]));
}
__device__ __forceinline__ float rdlane(float v, int l) { return __int_as_float(__builtin_amdgcn_readlane(__float_as_int(v), l)); }
__device__ __forceinline__ void glds16(const void* g, unsigned lds_base) {
  unsigned sv;
  asm volatile("s_mov_b32 %0, m0\n\ts_mov_b32 m0, %2\n\ts_nop 0\n\tglobal_load_lds_dwordx4 %1, off sc1\n\ts_mov_b32 m0, %0"
               : "=&s"(sv) : "v"(g), "s"(lds_base) : "memory");
}
__device__ __forceinline__ u32x4 ldg16(const void* p) { return *(const GAS u32x4*)p; }
__device__ __forceinline__ u32x2 tr_read(const LAS unsigned char* p) {
  return __builtin_bit_cast(u32x2, __builtin_amdgcn_ds_read_tr16_b64_v4i16((LAS s16x4*)p));
}
__device__ __forceinline__ int fresh_lane() {
  int l;
  asm volatile("v_mbcnt_lo_u32_b32 %0, -1, 0\n\tv_mbcnt_hi_u32_b32 %0, -1, %0" : "=v"(l));
  return l;
}
#define LD16(voff, soff) __builtin_amdgcn_raw_buffer_load_b128(rs, voff, soff, AUX_SC1)
#define LDF4(voff, soff) __builtin_bit_cast(f32x4, LD16(voff, soff))
#define LDMOD(voff, soff) ({ const f32x4 t_ = LDF4(voff, WS_MODP + (soff)) + LDF4(voff, WS_MODP + 8192 + (soff)); \
    const unsigned a_ = pack2(t_[0], t_[1]), b_ = pack2(t_[2], t_[3]); (f32x4){bflo(a_), bfhi(a_), bflo(b_), bfhi(b_)}; })
#define ST16(v, voff, soff) do { const u32x4 d_ = (v); __builtin_amdgcn_raw_buffer_store_b128(d_, rs, voff, soff, AUX_SC1); asm volatile("s_nop 1" :: "v"(d_)); } while (0)
__device__ __forceinline__ unsigned umax4(u32x4 d) { return max(max(d[0], d[1]), max(d[2], d[3])); }

__device__ __forceinline__ void ln_stats(const u32x4 (&xr)[8], float (&rs)[8], float (&nb)[8]) {
  float s[8], q[8];
#pragma unroll
  for (int i = 0; i < 8; ++i) {
    s[i] = dot2bf(xr[i][0], 0x3f803f80u, dot2bf(xr[i][1], 0x3f803f80u, dot2bf(xr[i][2], 0x3f803f80u, dot2bf(xr[i][3], 0x3f803f80u, 0.f))));
    q[i] = dot2bf(xr[i][0], xr[i][0], dot2bf(xr[i][1], xr[i][1], dot2bf(xr[i][2], xr[i][2], dot2bf(xr[i][3], xr[i][3], 0.f))));
  }
  float ts[4], tq[4];
#pragma unroll
  for (int i = 0; i < 4; ++i) { ts[i] = fold32(s[i], s[i + 4]); tq[i] = fold32(q[i], q[i + 4]); }
#pragma unroll
  for (int i = 0; i < 2; ++i) {
    const float mu = row16_sum(fold16(ts[i], ts[i + 2])) * (1.f / D_);
    const float e2 = row16_sum(fold16(tq[i], tq[i + 2])) * (1.f / D_);
    const float r = __builtin_amdgcn_rsqf(fmaxf(fmaf(-mu, mu, e2), 0.f) + LN_EPS);
    const float n = -mu * r;
#pragma unroll
    for (int p = 0; p < 4; ++p) { rs[i + 2 * p] = rdlane(r, 16 * p); nb[i + 2 * p] = rdlane(n, 16 * p); }
  }
}

#define SUB4(v, i) __builtin_shufflevector(v, v, 4 * (i), 4 * (i) + 1, 4 * (i) + 2, 4 * (i) + 3)
__device__ __forceinline__ unsigned lds_arrive(LAS unsigned char* p, int lane) {
  asm volatile("s_waitcnt lgkmcnt(0)" ::: "memory");
  unsigned old = 0;
  if (lane == 0) old = __hip_atomic_fetch_add((LAS unsigned*)p, 1u, __ATOMIC_RELAXED, __HIP_MEMORY_SCOPE_WORKGROUP);
  return __builtin_amdgcn_readfirstlane(old);
}
template <int N> __device__ __forceinline__ void lds_wait(LAS unsigned char* p) {
  unsigned v, t, n;
  asm volatile("s_mov_b32 %2, 0x20000\n"
               "1:\nds_read_b32 %0, %3\ns_waitcnt lgkmcnt(0)\nv_readfirstlane_b32 %1, %0\ns_cmp_ge_u32 %1, %4\ns_cbranch_scc1 2f\n"
               "s_sleep 1\ns_sub_u32 %2, %2, 1\ns_cmp_lg_u32 %2, 0\ns_cbranch_scc1 1b\n2:\n"
               : "=&v"(v), "=&s"(t), "=&s"(n) : "v"((unsigned)(uintptr_t)p), "i"(N) : "memory", "scc");
}
__device__ __forceinline__ void lds_arrive_flag(LAS unsigned char* p, unsigned char* flag, unsigned voff, unsigned val, unsigned mask) {
  unsigned v, t; u64 e;
  asm volatile("s_mov_b64 %2, exec\ns_mov_b64 exec, 1\nds_add_rtn_u32 %0, %3, %4\ns_waitcnt lgkmcnt(0)\nv_readfirstlane_b32 %1, %0\n"
               "s_cmp_lg_u32 %1, 7\ns_cbranch_scc1 1f\ns_mov_b32 exec_lo, %8\nglobal_store_dword %6, %7, %5 sc1\n1:\ns_mov_b64 exec, %2\n"
               : "=&v"(v), "=&s"(t), "=&s"(e) : "v"((unsigned)(uintptr_t)p), "v"(1u), "s"(flag), "v"(voff), "v"(val), "s"(mask) : "memory", "scc");
}
template <int CTRL> __device__ __forceinline__ unsigned dppu(unsigned v) { return __builtin_amdgcn_update_dpp(0u, v, CTRL, 0xf, 0xf, true); }
__device__ __forceinline__ unsigned wave_umax(unsigned t) {
  t = max(t, dppu<0xB1>(t)); t = max(t, dppu<0x4E>(t)); t = max(t, dppu<0x141>(t)); t = max(t, dppu<0x140>(t));
  auto r = __builtin_amdgcn_permlane16_swap(t, t, false, false);
  t = max(r[0], r[1]);
  auto r2 = __builtin_amdgcn_permlane32_swap(t, t, false, false);
  return max(r2[0], r2[1]);
}
__device__ __forceinline__ float knorm2(u32x4 k) { return dot2bf(k[0], k[0], dot2bf(k[1], k[1], dot2bf(k[2], k[2], dot2bf(k[3], k[3], 0.f)))); }

__device__ __forceinline__ void wait_flags(const unsigned char* ws, int kind, int first, int n, int lane) {
  const GAS unsigned* f = (const GAS unsigned*)(ws + WS_FLAG + (kind * 256 + first + (lane < n ? lane : 0)) * 128);
#pragma unroll 1
  for (int spins = 0; spins < SPIN_CAP; ++spins) {
    const unsigned v = __hip_atomic_load(f, RLX_AGENT);
    if (__all(v == 1u)) break;
    __builtin_amdgcn_s_sleep(1);
  }
  __builtin_amdgcn_fence(__ATOMIC_ACQUIRE, "wavefront");
}
#define PINS(x) asm volatile("" : "+s"(x))
#define DRAIN_BAR() do { SBAR(); asm volatile("s_waitcnt vmcnt(0) lgkmcnt(0)\n\ts_barrier" ::: "memory"); SBAR(); } while (0)
__device__ __forceinline__ void raise_flag(unsigned char* flag, int w) {
  unsigned a, b; u64 e;
  asm volatile("s_cmp_lg_u32 %3, 0\ns_cbranch_scc1 1f\ns_mov_b64 %2, exec\ns_mov_b64 exec, 1\nv_mov_b32 %0, 0\nv_mov_b32 %1, 1\n"
               "global_store_dword %0, %1, %4 sc1\ns_mov_b64 exec, %2\n1:\n" : "=&v"(a), "=&v"(b), "=&s"(e) : "s"(w), "s"(flag) : "memory", "scc");
}

#define ATT_TILE_A(KO, V1O, V0O, ADV, X1, X2) \
  "s_waitcnt lgkmcnt(0)\nv_mfma_f32_32x32x16_bf16 v[208:223], v[228:231], v[224:227], v[160:175]\n" \
  "ds_read_b128 v[228:231], v248 offset:" KO "\n" \
  "v_exp_f32 v192, v192\nv_exp_f32 v193, v193\nv_exp_f32 v194, v194\nv_cvt_pk_bf16_f32 v240, v192, v193\n" \
  X1 \
  "v_exp_f32 v195, v195\nv_exp_f32 v196, v196\nv_cvt_pk_bf16_f32 v241, v194, v195\nv_exp_f32 v197, v197\n" \
  "v_mfma_f32_32x32x16_bf16 v[176:191], v[236:239], v[244:247], v[176:191]\n" \
  "ds_read_b128 v[236:239], v249 offset:" V1O "\n" \
  ADV \
  "v_exp_f32 v198, v198\nv_cvt_pk_bf16_f32 v242, v196, v197\nv_exp_f32 v199, v199\nv_exp_f32 v200, v200\n" \
  "v_cvt_pk_bf16_f32 v243, v198, v199\nv_exp_f32 v201, v201\nv_exp_f32 v202, v202\nv_cvt_pk_bf16_f32 v244, v200, v201\n" \
  "v_mfma_f32_32x32x16_bf16 v[176:191], v[232:235], v[240:243], v[176:191]\n" \
  "ds_read_b128 v[232:235], v249 offset:" V0O "\n" \
  "v_exp_f32 v203, v203\nv_exp_f32 v204, v204\nv_exp_f32 v205, v205\nv_cvt_pk_bf16_f32 v245, v202, v203\n" \
  X2 \
  "v_exp_f32 v206, v206\nv_exp_f32 v207, v207\nv_cvt_pk_bf16_f32 v246, v204, v205\nv_cvt_pk_bf16_f32 v247, v206, v207\n"
#define ATT_TILE_B(KO, V1O, V0O, ADV, X1, X2) \
  "s_waitcnt lgkmcnt(0)\nv_mfma_f32_32x32x16_bf16 v[192:207], v[228:231], v[224:227], v[160:175]\n" \
  "ds_read_b128 v[228:231], v248 offset:" KO "\n" \
  "v_exp_f32 v208, v208\nv_exp_f32 v209, v209\nv_exp_f32 v210, v210\nv_cvt_pk_bf16_f32 v240, v208, v209\n" \
  X1 \
  "v_exp_f32 v211, v211\nv_exp_f32 v212, v212\nv_cvt_pk_bf16_f32 v241, v210, v211\nv_exp_f32 v213, v213\n" \
  "v_mfma_f32_32x32x16_bf16 v[176:191], v[236:239], v[244:247], v[176:191]\n" \
  "ds_read_b128 v[236:239], v249 offset:" V1O "\n" \
  ADV \
  "v_exp_f32 v214, v214\nv_cvt_pk_bf16_f32 v242, v212, v213\nv_exp_f32 v215, v215\nv_exp_f32 v216, v216\n" \
  "v_cvt_pk_bf16_f32 v243, v214, v215\nv_exp_f32 v217, v217\nv_exp_f32 v218, v218\nv_cvt_pk_bf16_f32 v244, v216, v217\n" \
  "v_mfma_f32_32x32x16_bf16 v[176:191], v[232:235], v[240:243], v[176:191]\n" \
  "ds_read_b128 v[232:235], v249 offset:" V0O "\n" \
  "v_exp_f32 v219, v219\nv_exp_f32 v220, v220\nv_exp_f32 v221, v221\nv_cvt_pk_bf16_f32 v245, v218, v219\n" \
  X2 \
  "v_exp_f32 v222, v222\nv_exp_f32 v223, v223\nv_cvt_pk_bf16_f32 v246, v220, v221\nv_cvt_pk_bf16_f32 v247, v222, v223\n"
#define ATT_ASM(a0, b0, a1, b1, a2, b2, a3, b3, a4, b4, a5, b5, a6, b6, a7, b7) \
  "s_nop 1\n" \
  ATT_TILE_A("2048", "512", "1024", "", a0, b0) \
  ATT_TILE_B("3072", "1536", "2048", "", a1, b1) \
  ATT_TILE_A("4096", "2560", "3072", "", a2, b2) \
  ATT_TILE_B("5120", "3584", "4096", "", a3, b3) \
  ATT_TILE_A("6144", "4608", "5120", "", a4, b4) \
  ATT_TILE_B("7168", "5632", "6144", "", a5, b5) \
  ATT_TILE_A("16384", "6656", "7168", "", a6, b6) \
  ATT_TILE_B("17408", "7680", "0", "v_add_u32 v249, v249, v250\nv_add_u32 v248, 0x4000, v248\n", a7, b7) \
  "s_waitcnt lgkmcnt(0)\ns_nop 3\n"

#define ATT_STATE "+{v[176:191]}"(o), "+{v[192:207]}"(sA), "+{v[228:231]}"(kf), "+{v[232:235]}"(v0), "+{v[236:239]}"(v1), \
                  "+{v[244:247]}"(p1), "+{v248}"(kp), "+{v249}"(vp)
#define ATT_IN "{v[160:175]}"(cin), "{v[224:227]}"(qf), "{v250}"(vstep)
#define ATT_CLOB "v208", "v209", "v210", "v211", "v212", "v213", "v214", "v215", "v216", "v217", "v218", "v219", "v220", "v221", "v222", \
                 "v223", "v240", "v241", "v242", "v243", "memory"
#define ATT_PLAIN() do { SBAR(); asm volatile(ATT_ASM("", "", "", "", "", "", "", "", "", "", "", "", "", "", "", "") \
    : ATT_STATE : ATT_IN : ATT_CLOB); SBAR(); } while (0)
#define GL(R, VO, OFF) "global_load_dwordx4 " R ", " VO ", %[sb] offset:" OFF " sc1\n"
#define DMA(VO, OFF) "global_load_lds_dwordx4 " VO ", %[sb] offset:" OFF " sc1\n"

__global__ void __launch_bounds__(512, 2)
fused_block(const u16* x, const u16* wq, const u16* wk, const u16* wv,
            const u16* __restrict__ wo, const u16* __restrict__ w1, const u16* __restrict__ w2, const u16* cvec,
            const u16* wmod, unsigned char* ws, u16* __restrict__ out) {
  __shared__ __attribute__((aligned(16))) unsigned char lds[L_END];
  LAS unsigned char* L = (LAS unsigned char*)lds;
  const unsigned lds0 = (unsigned)(uintptr_t)lds;
  const int bid = blockIdx.x, tid = threadIdx.x, lane = tid & 63, w = __builtin_amdgcn_readfirstlane(tid >> 6);
  const int rb = (bid & 7) * 4 + ((bid >> 3) & 3), m = bid >> 5;
  const int g = lane >> 4, c = lane & 15, hi = lane >> 5, c32 = lane & 31;
  const __amdgpu_buffer_rsrc_t rs = __builtin_amdgcn_make_buffer_rsrc(ws, 0, WS_END, 0x00020000);
  STAMP(0);

  const bool crit = bid < 64;
  if (crit) {
    if (w == 0 && lane < 32) ST16(((u32x4){SENT, SENT, SENT, SENT}), lane * 16, WS_KF + bid * 512);
    if (w == 2) ST16(((u32x4){SENT, SENT, SENT, SENT}), lane * 16, WS_KV + bid * 1024);
    if (w == 1 && lane < 12) __builtin_amdgcn_raw_buffer_store_b32(0u, rs, lane * 128, WS_FLAG + bid * 1536, AUX_SC1);
    CFENCE();
  }
  if (tid < 8) *(LAS unsigned*)(L + L_CNT + tid * 4) = 0u;
  const u64 epoch = *(const __attribute__((address_space(4))) u64*)(ws + WS_EPOCH) + 0x9E3779B97F4A7C15ull;
  const bool late = (bid >> 6) == 1, kvwg = bid >= 128;
  const unsigned l16 = lane * 16, l16b = lane * 16 + 8192;
#define BL16(D, R, V, S) asm volatile("buffer_load_dwordx4 %0, %1, %2, %3 offen" : "=&v"(D) : "v"(V), "s"(R), "s"(S) : "memory")
#define BL2(D, R, V, S) asm volatile("buffer_load_ushort %0, %1, %2, %3 offen" : "=&v"(D) : "v"(V), "s"(R), "s"(S) : "memory")
  u32x4 wm[4];
  unsigned cv[4];
  const unsigned mvo = (tid >> 3) * 6144 + (tid & 7) * 16, cvo = (tid >> 3) * 2;
  const __amdgpu_buffer_rsrc_t xs = __builtin_amdgcn_make_buffer_rsrc((void*)x, 0, 2048 * 512 * 2, 0x00020000);
#define XROW(i) (16 * ((((i) >> 1) + m) & 3) + 2 * w + ((i) & 1))
  u32x4 xr[8];
#define SILU(v) ((v) * __builtin_amdgcn_rcpf(1.f + __builtin_amdgcn_exp2f((v) * -1.4426950408889634f)))
#define MOD_PART(N) do { \
    float p[8] = {0.f, 0.f, 0.f, 0.f, 0.f, 0.f, 0.f, 0.f}; \
    _Pragma("unroll") for (int i = 0; i < N; ++i) { const unsigned sl = pack2(SILU(bflo(cv[i])), 0.f), sh_ = sl << 16; \
      _Pragma("unroll") for (int q = 0; q < 4; ++q) { p[2 * q] = dot2bf(wm[i][q], sl, p[2 * q]); p[2 * q + 1] = dot2bf(wm[i][q], sh_, p[2 * q + 1]); } } \
    float t0 = fold16(fold32(p[0], p[4]), fold32(p[2], p[6])), t1 = fold16(fold32(p[1], p[5]), fold32(p[3], p[7])); \
    t0 += dpp<0x128>(t0); t1 += dpp<0x128>(t1); \
    if (c < 8) *(LAS f32x2*)(L + L_RED + (w * 64 + c * 8 + 2 * g) * 4) = (f32x2){t0, t1}; } while (0)
#define MOD_SUM() ({ float s_ = 0.f; _Pragma("unroll") for (int i = 0; i < 8; ++i) s_ += *(const LAS float*)(L + L_RED + (i * 64 + lane) * 4); s_; })
  {
    const __amdgpu_buffer_rsrc_t ms = __builtin_amdgcn_make_buffer_rsrc((void*)wmod, 0, crit ? 512 * 3072 * 2 : 0, 0x00020000);
    const __amdgpu_buffer_rsrc_t cs = __builtin_amdgcn_make_buffer_rsrc((void*)cvec, 0, crit ? 512 * 2 : 0, 0x00020000);
    const int so = (bid >> 4) * 128 * 6144 + (bid & 15) * 128;
    BL16(wm[0], ms, mvo, so); BL16(wm[1], ms, mvo, so + 64 * 6144);
    BL2(cv[0], cs, cvo, (bid >> 4) * 256); BL2(cv[1], cs, cvo, (bid >> 4) * 256 + 128);
#pragma unroll
    for (int i = 0; i < 4; ++i) BL16(xr[i], xs, l16, (64 * rb + XROW(i)) * 1024);
    asm volatile("s_waitcnt vmcnt(4)" : "+v"(wm[0]), "+v"(wm[1]), "+v"(cv[0]), "+v"(cv[1]) :: "memory");
  }
  if (crit) {
    MOD_PART(2);
    __syncthreads();
    const float s = MOD_SUM();
    const u64 gr = (epoch & ~0xffffffull) | ((__float_as_uint(s) + 0x80u) >> 8);
    GAS u64* gp = (GAS u64*)(ws + WS_MODG + w * 32768 + (bid >> 4) * 8192 + ((bid & 15) * 64 + lane) * 8);
    __hip_atomic_store(gp, gr, RLX_AGENT);
    __hip_atomic_store(gp + 32768, gr, RLX_AGENT);
  }
  STAMP(1);
#pragma unroll
  for (int i = 4; i < 8; ++i) BL16(xr[i], xs, l16, (64 * rb + XROW(i)) * 1024);
  {
    u64 pc; unsigned clen;
    asm volatile("s_getpc_b64 %0\n.Lfb_code_lo:\ns_mov_b32 %1, .Lfb_code_hi-.Lfb_code_lo" : "=s"(pc), "=s"(clen));
    const __amdgpu_buffer_rsrc_t a0 = __builtin_amdgcn_make_buffer_rsrc(late ? (void*)wmod : (kvwg ? (void*)wk : (void*)pc), 0, late ? 512 * 3072 * 2 : (kvwg ? 16384 : clen), 0x00020000);
    const __amdgpu_buffer_rsrc_t a1 = __builtin_amdgcn_make_buffer_rsrc(late ? (void*)wmod : (void*)wv, 0, late ? 512 * 3072 * 2 : (kvwg ? 16384 : 0), 0x00020000);
    const __amdgpu_buffer_rsrc_t cs = __builtin_amdgcn_make_buffer_rsrc((void*)cvec, 0, late ? 512 * 2 : 0, 0x00020000);
    const unsigned vo = late ? mvo : w * 2048 + l16;
    const int s0 = late ? (m & 1) * 256 * 6144 + 2048 + (bid & 31) * 128 : (kvwg ? 0 : (bid >> 3) * 16384), sd = late ? 64 * 6144 : 1024, s2 = late ? s0 + 128 * 6144 : 0;
    BL16(wm[0], a0, vo, s0); BL16(wm[1], a0, vo, s0 + sd); BL16(wm[2], a1, vo, s2); BL16(wm[3], a1, vo, s2 + sd);
#pragma unroll
    for (int i = 0; i < 4; ++i) BL2(cv[i], cs, cvo, (m & 1) * 512 + i * 128);
  }
  asm volatile("s_waitcnt vmcnt(8)" : "+v"(xr[0]), "+v"(xr[1]), "+v"(xr[2]), "+v"(xr[3]), "+v"(xr[4]), "+v"(xr[5]), "+v"(xr[6]), "+v"(xr[7]) :: "memory");
  float rsd[8], nb[8];
  ln_stats(xr, rsd, nb);
  const int hh = w >> 1, rt = w & 1;
  asm volatile("s_waitcnt vmcnt(0)" : "+v"(wm[0]), "+v"(wm[1]), "+v"(wm[2]), "+v"(wm[3]), "+v"(cv[0]), "+v"(cv[1]), "+v"(cv[2]), "+v"(cv[3]) :: "memory");
  u32x4 kfr[4];
  {
    const unsigned ov = (c & 11) == 0 ? 0x3f803f80u : 0u;
    *(LAS u32x4*)(L + L_ONES + tid * 16) = (u32x4){ov, ov, ov, ov};
    if (kvwg) {
#pragma unroll
      for (int i = 0; i < 4; ++i) *(LAS u32x4*)(L + L_H1 + w * 4096 + i * 1024 + lane * 16) = wm[i];
      const LAS unsigned char* b0 = L + L_H1 + w * 4096 + (8 * g + (c >> 2)) * 32 + (c & 3) * 8;
      asm volatile("s_waitcnt lgkmcnt(0)" ::: "memory");
#pragma unroll
      for (int i = 0; i < 4; ++i) {
        const u32x2 lo = tr_read(b0 + i * 1024), hi2 = tr_read(b0 + i * 1024 + 128);
        kfr[i] = (u32x4){lo[0], lo[1], hi2[0], hi2[1]};
      }
    }
  }
  const u16* gsrc; int gld, gup, gbytes, gdst, gdd; unsigned gvo, gvo2;
  {
    const int pi = bid * 8 + w;
    int rbase, col, dcol;
    if (pi < 256) { const int mm = pi >> 5, ks = (pi >> 1) & 15, tp = pi & 1;
      gsrc = wo; gld = 1024; gup = 5; dcol = 4; gbytes = 512 * 512 * 2; gdst = WS_WO + pi * 2048; gdd = 1024;
      rbase = 16 * (2 * ks + (g >> 1)) + 4 * (g & 1); col = 64 * mm + 32 * tp + 8 * (c >> 2) + (c & 3); }
    else if (pi < 1152) { const int id = pi - 256, a = id >> 5, ks = id & 31;
      gsrc = w1; gld = 2 * F_; gup = 1; dcol = 32; gbytes = 512 * F_ * 2; gdst = WS_W1 + (a * 64 + ks) * 1024; gdd = 32768;
      rbase = 16 * ks + 8 * hi; col = 64 * a + c32; }
    else { const int id = pi - 1152, mm = id / 112, ks = id - mm * 112;
      const int kk = ks % 14 - ((7 * mm) >> 2);
      gsrc = w2; gld = 1024; gup = 5; dcol = 32; gbytes = 512 * F_ * 2; gdst = WS_W2 + (id - ks % 14 + (kk < 0 ? kk + 14 : kk)) * 2048; gdd = 1024;
      rbase = 16 * ks + 4 * hi; col = 64 * mm + 16 * ((c32 >> 2) & 1) + (c32 & 3) + 4 * (c32 >> 3); }
    gvo = rbase * gld + col * 2; gvo2 = gvo + dcol * 2; gup *= gld;
  }
  constexpr int NXH = 2;
  f32x2 xh[NXH][4];
#define XHAT(i, q) (__builtin_elementwise_fma((f32x2){bflo(xr[i][q]), bfhi(xr[i][q])}, (f32x2){rsd[i], rsd[i]}, (f32x2){nb[i], nb[i]}))
#pragma unroll
  for (int i = 0; i < NXH; ++i) {
#pragma unroll
    for (int q = 0; q < 4; ++q) xh[i][q] = XHAT(i, q);
    asm volatile("" : "+v"(xh[i][0]), "+v"(xh[i][1]), "+v"(xh[i][2]), "+v"(xh[i][3]));
  }
  u32x4 mg[4];
  asm volatile("" : "+s"(rsd[0]), "+s"(rsd[1]), "+s"(nb[6]), "+s"(nb[7]) :: "memory");
  SBAR();
#define BL16C(D, V, S) asm volatile("buffer_load_dwordx4 %0, %1, %2, %3 offen sc1" : "=&v"(D) : "v"(V), "s"(rs), "s"(S) : "memory")
#pragma unroll
  for (int i = 0; i < 4; ++i) BL16C(mg[i], tid * 16, WS_MODG + (bid & 15) * 32768 + i * 8192);
  {
    const __amdgpu_buffer_rsrc_t qs = __builtin_amdgcn_make_buffer_rsrc((void*)wq, 0, 512 * 512 * 2, 0x00020000);
    const int r0 = tid >> 3, f = ((r0 >> 1) & 1) | ((r0 >> 2) & 2);
    const unsigned qvo = r0 * 1024 + m * 128 + (((tid & 7) ^ (f << 1)) << 4);
#pragma unroll
    for (int i = 0; i < 8; ++i) {
      const int t = (i + 2 * (bid >> 3)) & 7;
      asm volatile("s_mov_b32 m0, %3\n\ts_nop 0\n\tbuffer_load_dwordx4 %0, %1, %2 offen lds" :: "v"(qvo), "s"(qs), "s"(t * 65536), "s"(lds0 + t * 8192 + w * 1024) : "memory");
    }
  }
  asm volatile("s_waitcnt vmcnt(8)" : "+v"(mg[0]), "+v"(mg[1]), "+v"(mg[2]), "+v"(mg[3]) :: "memory");
  STAMP(2);
  {
    const unsigned thi = (unsigned)(epoch >> 32), tlo = (unsigned)epoch;
#pragma unroll 1
    for (int spins = 0; spins < SPIN_CAP; ++spins) {
      unsigned bad = 0u;
#pragma unroll
      for (int i = 0; i < 4; ++i) bad |= (mg[i][1] ^ thi) | (mg[i][3] ^ thi) | (((mg[i][0] ^ tlo) | (mg[i][2] ^ tlo)) & 0xff000000u);
      if (__all(bad == 0u)) break;
      if (spins == 0) COUNT(5);
      __builtin_amdgcn_s_sleep(2);
      CFENCE();
#pragma unroll
      for (int i = 0; i < 4; ++i) mg[i] = LD16(tid * 16, WS_MODG + (bid & 15) * 32768 + i * 8192);
    }
    const float a0 = (__uint_as_float(mg[0][0] << 8) + __uint_as_float(mg[1][0] << 8)) + (__uint_as_float(mg[2][0] << 8) + __uint_as_float(mg[3][0] << 8));
    const float a1 = (__uint_as_float(mg[0][2] << 8) + __uint_as_float(mg[1][2] << 8)) + (__uint_as_float(mg[2][2] << 8) + __uint_as_float(mg[3][2] << 8));
    unsigned t = pack2(a0, a1);
    if (w >= 4) t = pack2(1.f + bflo(t), 1.f + bfhi(t));
    *(LAS unsigned*)(L + L_MODA + tid * 4) = t;
  }
  __syncthreads();
  STAMP(3);
  u32x4 wf[16];
  u32x16 ge;
  {
    const u32x4 sa = *(const LAS u32x4*)(L + L_MODA + lane * 16), ca = *(const LAS u32x4*)(L + L_MODA + 1024 + lane * 16);
    f32x2 sc[4], sh[4];
#pragma unroll
    for (int q = 0; q < 4; ++q) { sc[q] = (f32x2){bflo(ca[q]), bfhi(ca[q])}; sh[q] = (f32x2){bflo(sa[q]), bfhi(sa[q])}; }
#define H_ROW(i) do { u32x4 o_; \
      _Pragma("unroll") for (int q = 0; q < 4; ++q) { const f32x2 t_ = __builtin_elementwise_fma((i) < NXH ? xh[(i) < NXH ? (i) : 0][q] : XHAT(i, q), sc[q], sh[q]); o_[q] = pack2(t_[0], t_[1]); } \
      *(LAS u32x4*)(L + L_H1 + XROW(i) * HROW + lane * 16) = o_; } while (0)
    H_ROW(0); H_ROW(1);
    unsigned kn = 0u;
    if (kvwg) {
      __syncthreads();
      const int sub = m - 4;
      const LAS unsigned char* hp = L + L_H1 + (16 * sub + c) * HROW + g * 16 + w * 128;
      const u32x4 h0 = *(const LAS u32x4*)hp, h1 = *(const LAS u32x4*)(hp + 64);
      f32x4 ak = {0.f, 0.f, 0.f, 0.f}, av = ak;
      ak = mfma16(kfr[0], h0, ak); av = mfma16(h0, kfr[2], av);
      ak = mfma16(kfr[1], h1, ak); av = mfma16(h1, kfr[3], av);
      *(LAS f32x4*)(L + L_WKV + w * 2048 + lane * 16) = ak;
      *(LAS f32x4*)(L + L_WKV + w * 2048 + 1024 + lane * 16) = av;
      __syncthreads();
      if (w < 2) {
        ak = *(const LAS f32x4*)(L + L_WKV + w * 1024 + lane * 16);
#pragma unroll
        for (int i = 1; i < 8; ++i) ak += *(const LAS f32x4*)(L + L_WKV + i * 2048 + w * 1024 + lane * 16);
        const u32x2 ok = {min(pack2(ak[0], ak[1]), SENT - 1u), min(pack2(ak[2], ak[3]), SENT - 1u)};
        const int tile = 2 * rb + (sub >> 1), s = sub & 1;
        const int kvo = WS_KV + (tile >> 3) * 16384 + (tile & 7) * 1024 + (w ? 8192 + s * 512 : s * 256);
        __builtin_amdgcn_raw_buffer_store_b64(ok, rs, w ? (g & 1) * 256 + c * 16 + (g >> 1) * 8 : ((g >> 1) * 32 + c) * 16 + (g & 1) * 8, kvo, AUX_SC1);
        STAMP(7);
        if (w == 0) {
          float n2 = dot2bf(ok[0], ok[0], dot2bf(ok[1], ok[1], 0.f));
          n2 = fold32(n2, n2);
          n2 = fold16(n2, n2);
          kn = min(wave_umax(__float_as_uint(n2)), SENT - 1u);
        }
      }
    }
    {
      const __amdgpu_buffer_rsrc_t gs = __builtin_amdgcn_make_buffer_rsrc((void*)gsrc, 0, gbytes, 0x00020000);
      unsigned so = 0;
#define GA(A, B, STEP) "buffer_load_ushort v" #A ", %[g0], %[gs], %[so] offen\nbuffer_load_ushort v" #B ", %[g1], %[gs], %[so] offen\ns_add_u32 %[so], %[so], " STEP "\n"
      asm volatile(GA(32, 40, "%[gl]") GA(33, 41, "%[gl]") GA(34, 42, "%[gl]") GA(35, 43, "%[gu]") GA(36, 44, "%[gl]") GA(37, 45, "%[gl]") GA(38, 46, "%[gl]") GA(39, 47, "%[gl]")
                   : "=&{v[32:47]}"(ge), [so] "+s"(so) : [g0] "v"(gvo), [g1] "v"(gvo2), [gs] "s"(gs), [gl] "s"(gld), [gu] "s"(gup) : "memory", "scc");
    }
    H_ROW(2); H_ROW(3); H_ROW(4); H_ROW(5); H_ROW(6); H_ROW(7);
    if (late) MOD_PART(4);
    asm volatile("s_waitcnt vmcnt(16)" ::: "memory");
    if (kvwg && w < 2 && lane < 16) __builtin_amdgcn_raw_buffer_store_b32(kn, rs, lane * 1024, WS_KF + ((bid - 128) * 2 + w) * 4, AUX_SC1);
    __syncthreads();
    STAMP(4);
    if (late && w == 0)
      __builtin_amdgcn_raw_buffer_store_b32(__float_as_uint(MOD_SUM()), rs, lane * 4, WS_MODP + (m & 1) * 8192 + (bid & 31) * 256, AUX_SC1);
  }
  const int kvw = (w >> 1) * 16384 + (w & 1) * 4096;
  u32x4 qf, kvs[8];
  {
    f32x4 acc0 = {0.f, 0.f, 0.f, 0.f}, acc1 = acc0;
    const LAS unsigned char* hp = L + L_H1 + (32 * rt + c) * HROW + g * 16;
    const int q4 = c >> 2, p4 = c & 3;
    const LAS unsigned char* a0 = L + (8 * g + q4) * 128 + ((hh ^ (((q4 >> 1) & 1) | ((g & 1) << 1))) << 5) + p4 * 8;
    u32x4 b0[16], b1[16];
#define Q_RD(ks) do { const u32x2 lo = tr_read(a0 + (ks) * 4096), hi2 = tr_read(a0 + (ks) * 4096 + 512); wf[ks] = (u32x4){lo[0], lo[1], hi2[0], hi2[1]}; \
      b0[ks] = *(const LAS u32x4*)(hp + (ks) * 64); b1[ks] = *(const LAS u32x4*)(hp + 16 * HROW + (ks) * 64); } while (0)
    Q_RD(0); Q_RD(1); Q_RD(2);
    SBAR();
#pragma unroll
    for (int ks = 0; ks < 16; ++ks) {
      if (ks >= KV0 && ks < KV0 + 8) kvs[ks - KV0] = LD16(l16, WS_KV + kvw + ((ks - KV0) >> 2) * 8192 + ((ks - KV0) & 3) * 1024);
      if (ks + 3 < 16) Q_RD(ks + 3);
      acc0 = mfma16(wf[ks], b0[ks], acc0);
      acc1 = mfma16(wf[ks], b1[ks], acc1);
      SBAR();
    }
    auto r0 = __builtin_amdgcn_permlane16_swap(pack2(acc0[0] * QSCALE, acc0[1] * QSCALE), pack2(acc1[0] * QSCALE, acc1[1] * QSCALE), false, false);
    auto r1 = __builtin_amdgcn_permlane16_swap(pack2(acc0[2] * QSCALE, acc0[3] * QSCALE), pack2(acc1[2] * QSCALE, acc1[3] * QSCALE), false, false);
    qf = (u32x4){r0[0], r1[0], r0[1], r1[1]};
  }
  STAMP(5);
  float q2 = knorm2(qf);
  q2 = fold32(q2, q2);
  __syncthreads();
  {
    unsigned mx = 0u;
#pragma unroll
    for (int i = 0; i < 8; ++i) mx = max(mx, umax4(kvs[i]));
    if (__builtin_expect(__any(mx == SENT), 0)) {
#pragma unroll 1
      for (int spins = 0; spins < SPIN_CAP; ++spins) {
        unsigned bad = 0u;
#pragma unroll
        for (int i = 0; i < 8; ++i) bad |= __any(umax4(kvs[i]) == SENT) ? 1u << i : 0u;
        if (!bad) break;
        if (spins == 0) COUNT(3);
        __builtin_amdgcn_s_sleep(1);
        CFENCE();
#pragma unroll
        for (int i = 0; i < 8; ++i)
          if (bad >> i & 1u) kvs[i] = LD16(l16, WS_KV + kvw + (i >> 2) * 8192 + (i & 3) * 1024);
      }
    }
#pragma unroll
    for (int i = 0; i < 8; ++i) *(LAS u32x4*)(L + kvw + (i >> 2) * 8192 + (i & 3) * 1024 + lane * 16) = kvs[i];
    unsigned km = max(__float_as_uint(fold32(knorm2(kvs[0]), knorm2(kvs[1]))), __float_as_uint(fold32(knorm2(kvs[2]), knorm2(kvs[3]))));
    km = max(km, dppu<0xB1>(km)); km = max(km, dppu<0x4E>(km)); km = max(km, dppu<0x141>(km)); km = max(km, dppu<0x140>(km));
    if (c == 0) asm volatile("ds_max_u32 %0, %1" :: "v"(lds0 + L_CNT + 28), "v"(km) : "memory");
  }
  f32x16 o, cin;
#pragma unroll
  for (int r = 0; r < 16; ++r) o[r] = 0.f;
  unsigned kp = lds0 + lane * 16;
  unsigned vp = lds0 + ((c32 < 16) ? (8192 + hi * 256 + c32 * 16) : (L_ONES + (c32 - 16) * 16));
  unsigned vstep = (c32 < 16) ? 16384 : 0;
  u32x4 v1 = {0u, 0u, 0u, 0u}, p1 = {0u, 0u, 0u, 0u};
  asm volatile("" : "+{v[176:191]}"(o), "+{v[236:239]}"(v1), "+{v[244:247]}"(p1), "+{v248}"(kp), "+{v249}"(vp), "+{v250}"(vstep));
  u32x4 kfl;
  asm volatile("s_waitcnt lgkmcnt(0)\n\ts_barrier" ::: "memory");
  const float boundA = __builtin_amdgcn_sqrtf(q2 * *(const LAS float*)(L + L_CNT + 28)) * 1.0001f;
#pragma unroll
  for (int r = 0; r < 16; ++r) cin[r] = -boundA;
  STAMP(6);
  f32x16 sA;
  u32x4 kf, v0;
  asm volatile("ds_read_b128 v[228:231], v248\nds_read_b128 v[232:235], v249\ns_waitcnt lgkmcnt(1)\n"
               "v_mfma_f32_32x32x16_bf16 v[192:207], v[228:231], v[224:227], v[160:175]\nds_read_b128 v[228:231], v248 offset:1024\n"
               "s_nop 7\ns_nop 2\ns_waitcnt lgkmcnt(0)\n"
               : "=&{v[192:207]}"(sA), "=&{v[228:231]}"(kf), "=&{v[232:235]}"(v0) : "{v[160:175]}"(cin), "{v[224:227]}"(qf), "{v248}"(kp), "{v249}"(vp) : "memory");
  __builtin_amdgcn_s_setprio(PRIO0);
  const int nt = 7 * m + (w < 7 ? w : 0);
  {
#define PK(D, LO, HI) "v_lshl_or_b32 v" #D ", v" #HI ", 16, v" #LO "\n"
#define GS(R, B, OFF) "buffer_store_dwordx4 " R ", %[va], %[rs], " B " offen offset:" OFF " sc1\n"
#define GF(B, OFF) GS("v[48:51]", B, OFF)
#define SENT4 "v_mov_b32 v48, -1\nv_mov_b32 v49, -1\nv_mov_b32 v50, -1\nv_mov_b32 v51, -1\n"
    SBAR();
    asm volatile(SENT4
                 ATT_ASM("", "", "", "", "buffer_load_dwordx4 v[52:55], %[va], %[rs], %[kf] offen sc1\n", "",
                         PK(32, 32, 33) PK(33, 34, 35) PK(34, 36, 37) PK(35, 38, 39) GS("v[32:35]", "%[d0]", "0"), "", "", "",
                         PK(40, 40, 41) PK(41, 42, 43) PK(42, 44, 45) PK(43, 46, 47) GS("v[40:43]", "%[d1]", "0"), "", "", "",
                         GF("%[fo]", "0"), "")
                 "s_waitcnt vmcnt(3)\n"
                 : ATT_STATE, "+{v[32:47]}"(ge), "=&{v[52:55]}"(kfl)
                 : ATT_IN, [va] "v"(l16), [rs] "s"(rs), [kf] "s"(WS_KF + (bid & 15) * 1024), [d0] "s"(gdst), [d1] "s"(gdst + gdd),
                   [fo] "s"(WS_OB + ((rb * 2 + rt) * 32 + 4 * m + hh) * 1024)
                 : ATT_CLOB, "v48", "v49", "v50", "v51");
    SBAR();
  }
#pragma unroll 1
  for (int spins = 0; spins < SPIN_CAP && __any(umax4(kfl) == SENT); ++spins) {
    if (spins == 0) COUNT(4);
    __builtin_amdgcn_s_sleep(4);
    CFENCE();
    kfl = LD16(l16, WS_KF + (bid & 15) * 1024);
  }
  const unsigned kmB = umax4(kfl);
  STAMP(22);
  __builtin_amdgcn_s_setprio(PRIO1);
  {
    SBAR();
    asm volatile("s_mov_b32 m0, %[ld]\n"
                 ATT_ASM(DMA("%[va]", "0"), DMA("%[va]", "1024"), DMA("%[va]", "2048"), DMA("%[va]", "3072") "s_add_u32 m0, m0, 0x2000\n",
                         DMA("%[vb]", "0"), DMA("%[vb]", "1024"), DMA("%[vb]", "2048"), DMA("%[vb]", "3072"), "", "", "", "", "", "", "", "") "s_waitcnt vmcnt(0)\n"
                 : ATT_STATE
                 : ATT_IN, [va] "v"(l16), [vb] "v"(l16b), [sb] "s"(ws + WS_KV + 65536 + kvw), [ld] "s"(lds0 + 65536 + kvw) : ATT_CLOB, "scc");
    SBAR();
  }
  lds_arrive_flag(L + L_CNT, ws + WS_AUXF + bid * 4, l16 * 64, 0u, 0xffffu);
  STAMP(23);
  __builtin_amdgcn_s_setprio(PRIO2);
#define ATT_FILL(F0, F1, X) do { SBAR(); \
    asm volatile(SENT4 ATT_ASM(GF("%[f0]", "0"), "", "", "", "", "", GF("%[f1]", "0"), "", "", "", "", "", X, "", "", "") \
                 : ATT_STATE : ATT_IN, [va] "v"(l16), [rs] "s"(rs), [f0] "s"(F0), [f1] "s"(F1) : ATT_CLOB, "v48", "v49", "v50", "v51"); SBAR(); } while (0)
  ATT_FILL(WS_YB + ((rb * 2 + (w >> 2)) * 32 + 4 * m + (w & 3)) * 1024, WS_HB + (rb * 224 + 112 + 2 * nt) * 1024, GF("%[f1]", "1024"));
  lds_wait<8>(L + L_CNT);
  {
    const float boundB = fmaxf(__builtin_amdgcn_sqrtf(q2 * __uint_as_float(wave_umax(kmB))) * 1.0001f, boundA);
    const float dl = boundA - boundB, f = __builtin_amdgcn_exp2f(dl);
    o = mfma32(v1, p1, o);
    p1 = (u32x4){0u, 0u, 0u, 0u};
#pragma unroll
    for (int r = 0; r < 16; ++r) { sA[r] += dl; cin[r] = -boundB; }
#pragma unroll
    for (int r = 0; r < 9; ++r) o[r] *= f;
  }
  STAMP(24);
  __builtin_amdgcn_s_setprio(PRIO3);
  ATT_FILL(WS_HB + (rb * 224 + 2 * nt) * 1024, w < 7 ? WS_HB + (rb * 224 + 2 * nt + 1) * 1024 : WS_ST + rb * 8192 + m * 1024, "");
  lds_arrive(L + L_CNT + 8, lane);
  CFENCE();
  u32x4 af = LD16(l16, WS_AUXF + (bid & 15) * 1024);
  STAMP(25);
  __builtin_amdgcn_s_setprio(PRIO4);
  ATT_PLAIN();
#pragma unroll 1
  for (int spins = 0; spins < SPIN_CAP && __any(umax4(af) == SENT); ++spins) {
    if (spins == 0) COUNT(6);
    __builtin_amdgcn_s_sleep(8);
    CFENCE();
    af = LD16(l16, WS_AUXF + (bid & 15) * 1024);
  }
  if (bid == 0 && tid == 0) __hip_atomic_store((GAS u64*)(ws + WS_EPOCH), epoch, RLX_AGENT);
  f32x4 tb0, tb1;
  asm volatile("global_load_dwordx4 %0, %2, %4 sc1\nglobal_load_dwordx4 %1, %3, %4 sc1" : "=&v"(tb0), "=&v"(tb1) : "v"(l16), "v"(l16b), "s"(ws + WS_MODP + w * 1024) : "memory");
  u32x16 w1t[8];
  unsigned char* wpre = ws + WS_W1 + nt * 32768;
  u32x4 xin;
  asm volatile("global_load_dwordx4 %0, %1, %2" : "=&v"(xin) : "v"(c * 1024 + g * 16), "s"(x + (size_t)(64 * rb + 16 * (w & 3)) * D_ + 64 * m + 32 * (w >> 2)) : "memory");
#define ATT_W1(B, T0, T1, T2, T3) do { SBAR(); \
    asm volatile(ATT_ASM(GL("v[" #B "+0:" #B "+3]", "%[va]", "-4096"), GL("v[" #B "+4:" #B "+7]", "%[va]", "-3072"), \
                         GL("v[" #B "+8:" #B "+11]", "%[va]", "-2048"), GL("v[" #B "+12:" #B "+15]", "%[va]", "-1024"), \
                         GL("v[" #B "+16:" #B "+19]", "%[va]", "0"), GL("v[" #B "+20:" #B "+23]", "%[va]", "1024"), \
                         GL("v[" #B "+24:" #B "+27]", "%[va]", "2048"), GL("v[" #B "+28:" #B "+31]", "%[va]", "3072"), \
                         GL("v[" #B "+32:" #B "+35]", "%[vb]", "-4096"), GL("v[" #B "+36:" #B "+39]", "%[vb]", "-3072"), \
                         GL("v[" #B "+40:" #B "+43]", "%[vb]", "-2048"), GL("v[" #B "+44:" #B "+47]", "%[vb]", "-1024"), \
                         GL("v[" #B "+48:" #B "+51]", "%[vb]", "0"), GL("v[" #B "+52:" #B "+55]", "%[vb]", "1024"), \
                         GL("v[" #B "+56:" #B "+59]", "%[vb]", "2048"), GL("v[" #B "+60:" #B "+63]", "%[vb]", "3072")) \
                 : ATT_STATE, T0, T1, T2, T3 \
                 : ATT_IN, [va] "v"(l16), [vb] "v"(l16b), [sb] "s"(wpre + ((B) - 32) * 256 + 4096) : ATT_CLOB); SBAR(); } while (0)
  STAMP(26);
  __builtin_amdgcn_s_setprio(PRIO5);
  ATT_W1(32, "=&{v[32:47]}"(w1t[0]), "=&{v[48:63]}"(w1t[1]), "=&{v[64:79]}"(w1t[2]), "=&{v[80:95]}"(w1t[3]));
  asm volatile("s_waitcnt vmcnt(16)" : "+v"(tb0), "+v"(tb1), "+v"(xin) :: "memory");
  {
    const f32x4 t = tb0 + tb1;
    u32x2 tp = {pack2(t[0], t[1]), pack2(t[2], t[3])};
    if ((w >> 1) == 2) tp = (u32x2){pack2(1.f + bflo(tp[0]), 1.f + bfhi(tp[0])), pack2(1.f + bflo(tp[1]), 1.f + bfhi(tp[1]))};
    *(LAS u32x2*)(L + L_TAB + w * 512 + (l16 >> 1)) = tp;
  }
  lds_arrive(L + L_CNT + 24, lane);
  STAMP(27);
  __builtin_amdgcn_s_setprio(PRIO6);
  ATT_W1(96, "=&{v[96:111]}"(w1t[4]), "=&{v[112:127]}"(w1t[5]), "=&{v[128:143]}"(w1t[6]), "=&{v[144:159]}"(w1t[7]));
  lds_wait<8>(L + L_CNT + 8);
  STAMP(28);
  __builtin_amdgcn_s_setprio(PRIO7);
  {
    unsigned sv;
    SBAR();
    asm volatile("s_mov_b32 %[sv], m0\ns_mov_b32 m0, %[ld]\n"
                 ATT_ASM(DMA("%[va]", "0"), DMA("%[va]", "1024"), DMA("%[va]", "2048"), DMA("%[va]", "3072") "s_add_u32 m0, m0, 0x2000\n",
                         DMA("%[vb]", "0"), DMA("%[vb]", "1024"), DMA("%[vb]", "2048"), DMA("%[vb]", "3072"), "", "", "", "", "", "", "", "")
                 "s_mov_b32 m0, %[sv]\n"
                 : ATT_STATE, [sv] "=&s"(sv)
                 : ATT_IN, [va] "v"(l16), [vb] "v"(l16b), [sb] "s"(ws + WS_WO + m * 65536 + kvw), [ld] "s"(lds0 + kvw) : ATT_CLOB, "scc");
    SBAR();
  }
  __builtin_amdgcn_s_setprio(0);
  STAMP(29);
  DRAIN();
  u32x4 w1f[32];
  asm volatile("" : "={v[32:35]}"(w1f[0]), "={v[36:39]}"(w1f[1]), "={v[40:43]}"(w1f[2]), "={v[44:47]}"(w1f[3]) : "{v[32:47]}"(w1t[0]));
  asm volatile("" : "={v[48:51]}"(w1f[4]), "={v[52:55]}"(w1f[5]), "={v[56:59]}"(w1f[6]), "={v[60:63]}"(w1f[7]) : "{v[48:63]}"(w1t[1]));
  asm volatile("" : "={v[64:67]}"(w1f[8]), "={v[68:71]}"(w1f[9]), "={v[72:75]}"(w1f[10]), "={v[76:79]}"(w1f[11]) : "{v[64:79]}"(w1t[2]));
  asm volatile("" : "={v[80:83]}"(w1f[12]), "={v[84:87]}"(w1f[13]), "={v[88:91]}"(w1f[14]), "={v[92:95]}"(w1f[15]) : "{v[80:95]}"(w1t[3]));
  asm volatile("" : "={v[96:99]}"(w1f[16]), "={v[100:103]}"(w1f[17]), "={v[104:107]}"(w1f[18]), "={v[108:111]}"(w1f[19]) : "{v[96:111]}"(w1t[4]));
  asm volatile("" : "={v[112:115]}"(w1f[20]), "={v[116:119]}"(w1f[21]), "={v[120:123]}"(w1f[22]), "={v[124:127]}"(w1f[23]) : "{v[112:127]}"(w1t[5]));
  asm volatile("" : "={v[128:131]}"(w1f[24]), "={v[132:135]}"(w1f[25]), "={v[136:139]}"(w1f[26]), "={v[140:143]}"(w1f[27]) : "{v[128:143]}"(w1t[6]));
  asm volatile("" : "={v[144:147]}"(w1f[28]), "={v[148:151]}"(w1f[29]), "={v[152:155]}"(w1f[30]), "={v[156:159]}"(w1f[31]) : "{v[144:159]}"(w1t[7]));
  o = mfma32(v1, p1, o);
  {
  const int lane = fresh_lane(), tid = w * 64 + lane;
  const int g = lane >> 4, c = lane & 15, hi = lane >> 5, c32 = lane & 31;
  if (!__all(o[8] >= 5.421010862427522e-20f)) {
    f32x16 z;
#pragma unroll
    for (int r = 0; r < 16; ++r) { z[r] = 0.f; o[r] = 0.f; }
    float mrun = -INFINITY;
    const unsigned ov = (c32 >= 16 && (c & 11) == 0) ? 0x3f803f80u : 0u;
#pragma unroll 1
    for (int t = 0; t < 64; ++t) {
      const int tb = WS_KV + (t >> 3) * 16384 + (t & 7) * 1024;
      const u32x4 kk = LD16(lane * 16, tb);
      u32x4 va = LD16(hi * 256 + c * 16, tb + 8192);
      u32x4 vb = LD16(hi * 256 + c * 16, tb + 8192 + 512);
      if (c32 >= 16) { va = (u32x4){ov, ov, ov, ov}; vb = va; }
      const f32x16 s = mfma32(kk, qf, z);
      float tm = s[0];
#pragma unroll
      for (int r = 1; r < 16; ++r) tm = fmaxf(tm, s[r]);
      const float mn = fmaxf(mrun, max32(tm));
      const float al = __builtin_amdgcn_exp2f(mrun - mn);
      mrun = mn;
      u32x4 q0, q1;
#pragma unroll
      for (int q = 0; q < 4; ++q) {
        q0[q] = pack2(__builtin_amdgcn_exp2f(s[2 * q] - mn), __builtin_amdgcn_exp2f(s[2 * q + 1] - mn));
        q1[q] = pack2(__builtin_amdgcn_exp2f(s[8 + 2 * q] - mn), __builtin_amdgcn_exp2f(s[8 + 2 * q + 1] - mn));
      }
#pragma unroll
      for (int r = 0; r < 16; ++r) o[r] *= al;
      o = mfma32(va, q0, o);
      o = mfma32(vb, q1, o);
    }
  }
  STAMP(9);
#define CANON(v) do { _Pragma("unroll") for (int q_ = 0; q_ < 4; ++q_) v[q_] = min(v[q_], SENT - 1u); } while (0)
  {
    const float rl = __builtin_amdgcn_rcpf(o[8]);
    u32x4 of;
#pragma unroll
    for (int q = 0; q < 4; ++q) of[q] = pack2(o[2 * q] * rl, o[2 * q + 1] * rl);
    CANON(of);
    ST16(of, lane * 16, WS_OB + ((rb * 2 + rt) * 32 + 4 * m + hh) * 1024);
  }
#define W1FRAG(ks) w1f[ks]
  f32x16 uv;
#pragma unroll
  for (int r = 0; r < 16; ++r) uv[r] = 0.f;
  const LAS unsigned char* tp = L + L_TAB + 2048 - (c32 & 1) * 1024 + hi * 16;
#define UV_PART(K0, K1) do { u32x4 tf_[K1 - K0]; SBAR(); \
    _Pragma("unroll") for (int ks = K0; ks < K1; ++ks) tf_[ks - K0] = *(const LAS u32x4*)(tp + ks * 32); \
    SBAR(); \
    _Pragma("unroll") for (int ks = K0; ks < K1; ++ks) uv = mfma32(W1FRAG(ks), tf_[ks - K0], uv); \
    PIN(uv); SBAR(); } while (0)
  const bool early = lds_arrive(L + L_CNT + 16, lane) < 4u;
  lds_wait<8>(L + L_CNT + 24);
  UV_PART(0, 16);
  if (early) UV_PART(16, 32);
  const int rs4 = w & 3, kh = w >> 2;
  const int ecol = 64 * m + 32 * kh + 8 * g;
#define W2OFF(v, i) (WS_W2 + (m * 112 + 14 * (v)) * 2048 + (i) * 1024)
  u32x4 x1p;
  {
    u32x4 bf[8];
    unsigned char* obp = ws + WS_OB + ((rb * 2 + (rs4 >> 1)) * 32 + 16 * kh) * 1024 + (rs4 & 1) * 256 + 4096;
    unsigned char* fl = ws + WS_FLAG + (rb * 8 + m) * 128;
    unsigned va = g * 512 + c * 16, vb = va + 8192;
    PINS(obp); PINS(fl); PIN(va); PIN(vb);
#define O_LOAD() asm volatile(GL("%0", "%[va]", "-4096") GL("%1", "%[va]", "-2048") GL("%2", "%[va]", "0") GL("%3", "%[va]", "2048") \
                              GL("%4", "%[vb]", "-4096") GL("%5", "%[vb]", "-2048") GL("%6", "%[vb]", "0") GL("%7", "%[vb]", "2048") \
      : "=&v"(bf[0]), "=&v"(bf[1]), "=&v"(bf[2]), "=&v"(bf[3]), "=&v"(bf[4]), "=&v"(bf[5]), "=&v"(bf[6]), "=&v"(bf[7]) \
      : [va] "v"(va), [vb] "v"(vb), [sb] "s"(obp) : "memory")
    f32x4 acc[4];
    unsigned wpo = kh * 32768 + lane * 16;
    PIN(wpo);
#define WO_RD(n) (*(const LAS u32x4*)(L + wpo + (n) * 1024))
    DRAIN_BAR();
    STAMP(10);
    raise_flag(fl, w);
#pragma unroll 1
    for (int pass = 0; pass < 64; ++pass) {
      O_LOAD();
      SBAR();
#pragma unroll
      for (int t = 0; t < 4; ++t) acc[t] = (f32x4){0.f, 0.f, 0.f, 0.f};
      u32x4 wq4[32];
      unsigned mx = 0u;
#pragma unroll
      for (int n = 0; n < WRD; ++n) wq4[n] = WO_RD(n);
      SBAR();
#pragma unroll
      for (int n = 0; n < 32; ++n) {
        if (n + WRD < 32) wq4[n + WRD] = WO_RD(n + WRD);
        if ((n & 3) == 0) asm volatile("s_waitcnt vmcnt(%1)" : "+v"(bf[n >> 2]) : "i"(7 - n / 4) : "memory");
        acc[n & 3] = mfma16(wq4[n], bf[n >> 2], acc[n & 3]);
        if ((n & 3) == 1) mx = max(mx, umax4(bf[n >> 2]));
        SBAR();
      }
      if (__builtin_expect(!__any(mx == SENT), 1)) break;
      COUNT(0);
      wait_flags(ws, 0, rb * 8 + 4 * kh, 4, lane);
    }
    STAMP(11);
    LAS f32x4* ex = (LAS f32x4*)(L + 65536);
    ex[(w * 2 + 0) * 64 + lane] = kh ? acc[0] : acc[2];
    ex[(w * 2 + 1) * 64 + lane] = kh ? acc[1] : acc[3];
    const u32x4 ga = *(const LAS u32x4*)(L + L_TAB + ecol * 2), cb = *(const LAS u32x4*)(L + L_TAB + 2048 + ecol * 2);
    __syncthreads();
    const f32x4 r0 = ex[((w ^ 4) * 2 + 0) * 64 + lane], r1 = ex[((w ^ 4) * 2 + 1) * 64 + lane];
    const f32x4 a0 = (kh ? acc[2] : acc[0]) + r0, a1 = (kh ? acc[3] : acc[1]) + r1;
    x1p[0] = pack2(fmaf(bflo(ga[0]), a0[0], bflo(xin[0])), fmaf(bfhi(ga[0]), a0[1], bfhi(xin[0])));
    x1p[1] = pack2(fmaf(bflo(ga[1]), a0[2], bflo(xin[1])), fmaf(bfhi(ga[1]), a0[3], bfhi(xin[1])));
    x1p[2] = pack2(fmaf(bflo(ga[2]), a1[0], bflo(xin[2])), fmaf(bfhi(ga[2]), a1[1], bfhi(xin[2])));
    x1p[3] = pack2(fmaf(bflo(ga[3]), a1[2], bflo(xin[3])), fmaf(bfhi(ga[3]), a1[3], bfhi(xin[3])));
    u32x4 y;
#pragma unroll
    for (int q = 0; q < 4; ++q) y[q] = pack2(bflo(x1p[q]) * bflo(cb[q]), bfhi(x1p[q]) * bfhi(cb[q]));
    CANON(y);
    ST16(y, g * 512 + c * 16, WS_YB + ((rb * 2 + (rs4 >> 1)) * 32 + 4 * m + 2 * kh) * 1024 + (rs4 & 1) * 256);
    float st = fold32(dot2bf(x1p[0], 0x3f803f80u, dot2bf(x1p[1], 0x3f803f80u, dot2bf(x1p[2], 0x3f803f80u, dot2bf(x1p[3], 0x3f803f80u, 0.f)))), knorm2(x1p));
    st = fold16(st, st);
    if ((g & 1) == 0)
      __builtin_amdgcn_raw_buffer_store_b32(min(__float_as_uint(st), SENT - 1u), rs, c * 8 + hi * 4, WS_ST + rb * 8192 + (2 * m + kh) * 512 + rs4 * 128, AUX_SC1);
  }
  PIN(x1p);
  if (!early) UV_PART(16, 32);
  STAMP(12);

#define NW2V 19
#define RDV 6
  constexpr int NW2 = NW2V, RD = RDV;
  u32x4 w2f[28];
  int hoff[14];
  {
    const int h0 = WS_HB + (rb * 224 + 14 * w) * 1024;
    int t = h0 + ((7 * m) >> 2) * 1024;
#pragma unroll
    for (int n = 0; n < 14; ++n) { PINS(t); hoff[n] = t; t += 1024; t = t == h0 + 14336 ? h0 : t; }
  }
  {
    u32x4 yr[8], sp;
    int yo = WS_YB + rb * 65536 + w * 8192, so = WS_ST + rb * 8192 + w * 64;
    unsigned char* fl = ws + WS_FLAG + (256 + rb * 8 + m) * 128;
    unsigned va = lane * 16, vb = va + 4096, vs = (lane >> 2) * 512 + (lane & 3) * 16;
    PINS(yo); PINS(so); PINS(fl); PIN(va); PIN(vb); PIN(vs);
#define Y_LOAD() do { CFENCE(); _Pragma("unroll") for (int i = 0; i < 8; ++i) yr[i] = LD16((i < 4 ? va : vb) + (i & 3) * 1024, yo); sp = LD16(vs, so); } while (0)
    DRAIN_BAR();
    STAMP(13);
    Y_LOAD();
    raise_flag(fl, w);
    SBAR();
#pragma unroll 1
    for (int pass = 0; pass < 64; ++pass) {
      unsigned mx = 0u;
#pragma unroll
      for (int i = 0; i < 8; ++i) { *(LAS u32x4*)(L + (w * 8 + i) * 1024 + lane * 16) = yr[i]; mx = max(mx, umax4(yr[i])); }
      mx = max(mx, umax4(sp));
      if (__builtin_expect(!__any(mx == SENT), 1)) break;
      COUNT(1);
      wait_flags(ws, 1, rb * 8, 8, lane);
      Y_LOAD();
    }
    STAMP(14);
    float s1 = fold32(__uint_as_float(sp[0]), __uint_as_float(sp[2])), s2 = fold32(__uint_as_float(sp[1]), __uint_as_float(sp[3]));
    s1 = fold16(s1, s1); s2 = fold16(s2, s2);
    s1 += dpp<0x128>(s1); s2 += dpp<0x128>(s2);
    s1 += dpp<0x124>(s1); s2 += dpp<0x124>(s2);
    const float mu = s1 * (1.f / D_);
    const float rstd = __builtin_amdgcn_rsqf(fmaxf(fmaf(-mu, mu, s2 * (1.f / D_)), 0.f) + LN_EPS);
    if (c32 < 4) *(LAS f32x2*)(L + L_STAT + (8 * w + 2 * c32 + hi) * 8) = (f32x2){rstd, -mu};
    __syncthreads();
    STAMP(15);
    if (w < 7) {
      const LAS unsigned char* hp = L + lane * 16;
      const f32x2 st0 = *(const LAS f32x2*)(L + L_STAT + c32 * 8), st1 = *(const LAS f32x2*)(L + L_STAT + 256 + c32 * 8);
      f32x16 acc0, acc1;
#pragma unroll
      for (int r = 0; r < 16; ++r) { acc0[r] = 0.f; acc1[r] = 0.f; }
      u32x4 bq[64];
      u32x4 h0, h1;
      const int ho = WS_HB + (rb * 224 + 2 * nt) * 1024;
#define PRE(A, r, ST) fmaf(fmaf(dpp<0xA0>(uv[r]), ST[1], A[r]), ST[0], dpp<0xF5>(uv[r]))
#define HPK(A, q, ST) ({ const float a_ = PRE(A, 2 * (q), ST), b_ = PRE(A, 2 * (q) + 1, ST); min(pack2(SILU(a_), SILU(b_)), SENT - 1u); })
#pragma unroll
      for (int n = 0; n < RD; ++n) bq[n] = *(const LAS u32x4*)(hp + n * 1024);
      SBAR();
#pragma unroll
      for (int n = 0; n < 64; ++n) {
        if (n + RD < 64) bq[n + RD] = *(const LAS u32x4*)(hp + (n + RD) * 1024);
        if (n < 32) acc0 = mfma32(W1FRAG(n & 31), bq[n], acc0); else acc1 = mfma32(W1FRAG(n & 31), bq[n], acc1);
        if ((n & 3) == 0 && 11 + n / 4 < NW2) w2f[11 + n / 4] = LD16(lane * 16, W2OFF(w, 11 + n / 4));
        if (n >= 34 && n < 50 && (n & 1) == 0) {
          const int q = (n - 34) / 2;
          const unsigned t = HPK(acc0, q, st0);
          if (q < 4) { h0[q] = t; PIN(h0); } else { h1[q - 4] = t; PIN(h1); }
        }
        if (n == 42) ST16(h0, lane * 16, ho);
        if (n == 50) ST16(h1, lane * 16, ho + 1024);
        if (n >= 36 && n % 3 == 0 && NW2 + (n - 36) / 3 < 28) w2f[NW2 + (n - 36) / 3] = LD16(lane * 16, W2OFF(w, NW2 + (n - 36) / 3));
        SBAR();
      }
      PIN(uv);
#pragma unroll
      for (int q = 0; q < 4; ++q) h0[q] = HPK(acc1, q, st1);
      ST16(h0, lane * 16, ho + 112 * 1024);
#pragma unroll
      for (int q = 0; q < 4; ++q) h1[q] = HPK(acc1, 4 + q, st1);
      ST16(h1, lane * 16, ho + 113 * 1024);
    } else {
#pragma unroll 1
      for (int v = 0; v < 8; ++v) {
        asm volatile("s_mov_b32 m0, %[ld]\ns_nop 0\n" DMA("%[va]", "0") DMA("%[va]", "1024") DMA("%[va]", "2048") DMA("%[va]", "3072") "s_add_u32 m0, m0, 0x1000\ns_nop 0\n"
                     DMA("%[vb]", "0") DMA("%[vb]", "1024") DMA("%[vb]", "2048") DMA("%[vb]", "3072") "s_add_u32 m0, m0, 0x1000\ns_nop 0\n"
                     DMA("%[vc]", "0") DMA("%[vc]", "1024") DMA("%[vc]", "2048")
                     :: [va] "v"(lane * 16), [vb] "v"(lane * 16 + 4096), [vc] "v"(lane * 16 + 8192), [sb] "s"(ws + W2OFF(v, 0)), [ld] "s"(lds0 + L_W2 + v * 11264)
                     : "memory", "scc");
      }
#pragma unroll
      for (int i = 11; i < 28; ++i) { w2f[i] = LD16(lane * 16, W2OFF(7, i)); __builtin_amdgcn_s_sleep(1); SBAR(); }
      DRAIN();
    }
  }
  STAMP(16);

  {
    constexpr int WIN = 14;
    unsigned va = lane * 16, vb = va + 112 * 1024;
    PIN(va); PIN(vb);
    u32x4 hf[WIN];
    SBAR();
    asm volatile("s_waitcnt lgkmcnt(0)\n\ts_barrier" ::: "memory");
    SBAR();
#pragma unroll
    for (int n = 0; n < WIN; ++n) hf[n] = LD16(va, hoff[n]);
    SBAR();
    LAS unsigned char* rp = L + w * 16384 + c32 * 256;
    {
      unsigned pa = L_W2 + w * 11264 + va;
      PIN(pa);
#pragma unroll
      for (int i = 0; i < 11; ++i) w2f[i] = *(const LAS u32x4*)(L + pa + i * 1024);
    }
    lds_arrive(L + L_CNT + 12, lane);
#pragma unroll 1
    for (int pass = 0; pass < 64; ++pass) {
      f32x16 acc[2][2];
#pragma unroll
      for (int r = 0; r < 16; ++r) { acc[0][0][r] = 0.f; acc[0][1][r] = 0.f; acc[1][0][r] = 0.f; acc[1][1][r] = 0.f; }
      unsigned mx = 0u;
      SBAR();
#define RED_WR(R2, ct, q) *(LAS f32x4*)(rp + (R2) * 8192 + (((8 * (ct) + 4 * hi + (q)) ^ (c32 & 15)) << 4)) = \
        (f32x4){acc[R2][ct][4 * (q)], acc[R2][ct][4 * (q) + 1], acc[R2][ct][4 * (q) + 2], acc[R2][ct][4 * (q) + 3]}
#pragma unroll
      for (int n = 0; n < 28; ++n) {
        const u32x4 cur = hf[n % WIN];
        acc[n / 14][0] = mfma32(w2f[2 * (n % 14)], cur, acc[n / 14][0]);
        acc[n / 14][1] = mfma32(w2f[2 * (n % 14) + 1], cur, acc[n / 14][1]);
        mx = max(mx, umax4(cur));
        if (n < 14) hf[n] = LD16(vb, hoff[n]);
        if (n == 1) lds_arrive_flag(L + L_CNT + 20, ws + WS_FLAG + (2 * 256 + rb * 8 + m) * 128, 0u, 1u, 1u);
        if (n == 13) STAMP(17);
        if (n == 15) lds_wait<8>(L + L_CNT + 12);
        if (n >= 16 && n < 24) RED_WR(0, (n - 16) >> 2, (n - 16) & 3);
        SBAR();
      }
      STAMP(18);
#pragma unroll
      for (int i = 0; i < 8; ++i) RED_WR(1, i >> 2, i & 3);
      if (__builtin_expect(!__any(mx == SENT), 1)) break;
      COUNT(2);
      wait_flags(ws, 2, rb * 8 + w, 1, lane);
#pragma unroll
      for (int n = 0; n < WIN; ++n) hf[n] = LD16(va, hoff[n]);
    }
    STAMP(19);
    const int lane = fresh_lane(), g = lane >> 4, c = lane & 15;
    const int erow = 64 * rb + 16 * rs4 + c, ecol = 64 * m + 32 * kh + 8 * g;
    const u32x4 gb = *(const LAS u32x4*)(L + L_TAB + 3072 + ecol * 2);
    __syncthreads();
    f32x4 a0 = {0.f, 0.f, 0.f, 0.f}, a1 = a0;
#pragma unroll
    for (int p = 0; p < 8; ++p) {
      a0 += *(const LAS f32x4*)(L + p * 16384 + (16 * rs4 + c) * 256 + (((8 * kh + 2 * g) ^ c) << 4));
      a1 += *(const LAS f32x4*)(L + p * 16384 + (16 * rs4 + c) * 256 + (((8 * kh + 2 * g + 1) ^ c) << 4));
    }
    u32x4 xo;
    xo[0] = pack2(fmaf(bflo(gb[0]), a0[0], bflo(x1p[0])), fmaf(bfhi(gb[0]), a0[1], bfhi(x1p[0])));
    xo[1] = pack2(fmaf(bflo(gb[1]), a0[2], bflo(x1p[1])), fmaf(bfhi(gb[1]), a0[3], bfhi(x1p[1])));
    xo[2] = pack2(fmaf(bflo(gb[2]), a1[0], bflo(x1p[2])), fmaf(bfhi(gb[2]), a1[1], bfhi(x1p[2])));
    xo[3] = pack2(fmaf(bflo(gb[3]), a1[2], bflo(x1p[3])), fmaf(bfhi(gb[3]), a1[3], bfhi(x1p[3])));
    const __amdgpu_buffer_rsrc_t os = __builtin_amdgcn_make_buffer_rsrc(out, 0, 2048 * 512 * 2, 0x00020000);
    __builtin_amdgcn_raw_buffer_store_b128(xo, os, (erow * D_ + ecol) * 2, 0, AUX_SC1);
  }
  STAMP(20);
  asm volatile(".Lfb_code_hi:");
  }
}

extern "C" void kernel_launch(void* const* d_in, const int* in_sizes, int n_in, void* d_out, int out_size, void* d_ws, size_t ws_size,
                              hipStream_t stream) {
  fused_block<<<256, 512, 0, stream>>>((const u16*)d_in[0], (const u16*)d_in[1], (const u16*)d_in[2], (const u16*)d_in[3],
                                       (const u16*)d_in[4], (const u16*)d_in[6], (const u16*)d_in[7], (const u16*)d_in[8],
                                       (const u16*)d_in[9], (unsigned char*)d_ws, (u16*)d_out);
}
```

```cpp
#include <hip/hip_runtime.h>
#include <hip/hip_bf16.h>
#include <stdint.h>

typedef __bf16 bf16x8 __attribute__((ext_vector_type(8)));
typedef __bf16 bf16x2 __attribute__((ext_vector_type(2)));
typedef float f32x2 __attribute__((ext_vector_type(2)));
typedef float f32x4 __attribute__((ext_vector_type(4)));
typedef float f32x16 __attribute__((ext_vector_type(16)));
typedef unsigned u32x4 __attribute__((ext_vector_type(4)));
typedef unsigned u32x2 __attribute__((ext_vector_type(2)));
typedef unsigned u32x16 __attribute__((ext_vector_type(16)));
typedef short s16x4 __attribute__((ext_vector_type(4)));
typedef unsigned short u16;
typedef unsigned long long u64;
#define LAS __attribute__((address_space(3)))
#define GAS __attribute__((address_space(1)))
#define RLX_AGENT __ATOMIC_RELAXED, __HIP_MEMORY_SCOPE_AGENT
#define SBAR() __builtin_amdgcn_sched_barrier(0)
#define PIN(x) asm volatile("" : "+v"(x))
#define DRAIN() asm volatile("s_waitcnt vmcnt(0)" ::: "memory")
#define CFENCE() asm volatile("" ::: "memory")

constexpr int D_ = 512, F_ = 1792;
constexpr float QSCALE = 0.25f * 1.4426950408889634f;
constexpr float LN_EPS = 1e-5f;
constexpr int AUX_SC1 = 16;
constexpr unsigned SENT = 0xffffffffu;
constexpr int SPIN_CAP = 1 << 17;

constexpr int WS_EPOCH = 0;
constexpr int WS_KF = 163840;
constexpr int WS_AUXF = 180224;
constexpr int WS_FLAG = 16384;
constexpr int WS_MODP = 131072;
constexpr int WS_KV = 262144;
constexpr int WS_MODG = 393216;
constexpr int WS_OB = 1310720;
constexpr int WS_ST = 917504;
constexpr int WS_YB = WS_OB + 2097152;
constexpr int WS_HB = WS_YB + 2097152;
constexpr int WS_WO = WS_HB + 7340032;
constexpr int WS_W1 = WS_WO + 512 * 1024;
constexpr int WS_W2 = WS_W1 + 1792 * 1024;
constexpr int WS_END = WS_W2 + 1792 * 1024;
#define STAMP(i)
#define COUNT(k)
#define STAMPW(i)

constexpr int HROW = 1040;
constexpr int L_H1 = 65536;
constexpr int L_ONES = 132096;
constexpr int L_WKV = 140288;
constexpr int L_MODA = 156672;
constexpr int L_RED = 158720;
constexpr int L_CNT = 160768;
constexpr int L_END = 160896;
constexpr int L_W2 = 65536;
constexpr int L_TAB = L_MODA;
constexpr int L_STAT = 155648;
#define KV0 2
#define WRD 6
#define PRIO0 3
#define PRIO1 2
#define PRIO2 1
#define PRIO3 0
#define PRIO4 3
#define PRIO5 3
#define PRIO6 2
#define PRIO7 1

__device__ __forceinline__ unsigned pack2(float a, float b) {
  f32x2 v = {a, b};
  return __builtin_bit_cast(unsigned, __builtin_convertvector(v, bf16x2));
}
__device__ __forceinline__ float bflo(unsigned u) { return __uint_as_float(u << 16); }
__device__ __forceinline__ float bfhi(unsigned u) { return __uint_as_float(u & 0xffff0000u); }
__device__ __forceinline__ float bfr(float a) { return bflo(pack2(a, 0.f)); }
__device__ __forceinline__ f32x16 mfma32(u32x4 a, u32x4 b, f32x16 c) {
  return __builtin_amdgcn_mfma_f32_32x32x16_bf16(__builtin_bit_cast(bf16x8, a), __builtin_bit_cast(bf16x8, b), c, 0, 0, 0);
}
__device__ __forceinline__ f32x4 mfma16(u32x4 a, u32x4 b, f32x4 c) {
  return __builtin_amdgcn_mfma_f32_16x16x32_bf16(__builtin_bit_cast(bf16x8, a), __builtin_bit_cast(bf16x8, b), c, 0, 0, 0);
}
__device__ __forceinline__ float dot2bf(unsigned a, unsigned b, float c) {
  return __builtin_amdgcn_fdot2_f32_bf16(__builtin_bit_cast(bf16x2, a), __builtin_bit_cast(bf16x2, b), c, false);
}
template <int CTRL> __device__ __forceinline__ float dpp(float v) {
  return __int_as_float(__builtin_amdgcn_update_dpp(0, __float_as_int(v), CTRL, 0xf, 0xf, true));
}
__device__ __forceinline__ float row16_sum(float t) { t += dpp<0xB1>(t); t += dpp<0x4E>(t); t += dpp<0x141>(t); t += dpp<0x140>(t); return t; }
__device__ __forceinline__ float row16_max(float t) { t = fmaxf(t, dpp<0xB1>(t)); t = fmaxf(t, dpp<0x4E>(t)); t = fmaxf(t, dpp<0x141>(t)); t = fmaxf(t, dpp<0x140>(t)); return t; }
__device__ __forceinline__ float fold32(float a, float b) {
  auto r = __builtin_amdgcn_permlane32_swap(__float_as_uint(a), __float_as_uint(b), false, false);
  return __uint_as_float(r[0]) + __uint_as_float(r[1]);
}
__device__ __forceinline__ float fold16(float a, float b) {
  auto r = __builtin_amdgcn_permlane16_swap(__float_as_uint(a), __float_as_uint(b), false, false);
  return __uint_as_float(r[0]) + __uint_as_float(r[1]);
}
__device__ __forceinline__ float max32(float a) {
  auto r = __builtin_amdgcn_permlane32_swap(__float_as_uint(a), __float_as_uint(a), false, false);
  return fmaxf(__uint_as_float(r[0]), __uint_as_float(r[1]));
}
__device__ __forceinline__ float max16(float a) {
  auto r = __builtin_amdgcn_permlane16_swap(__float_as_uint(a), __float_as_uint(a), false, false);
  return fmaxf(__uint_as_float(r[0]), __uint_as_float(r[1]));
}
__device__ __forceinline__ float rdlane(float v, int l) { return __int_as_float(__builtin_amdgcn_readlane(__float_as_int(v), l)); }
__device__ __forceinline__ void glds16(const void* g, unsigned lds_base) {
  unsigned sv;
  asm volatile("s_mov_b32 %0, m0\n\ts_mov_b32 m0, %2\n\ts_nop 0\n\tglobal_load_lds_dwordx4 %1, off sc1\n\ts_mov_b32 m0, %0"
               : "=&s"(sv) : "v"(g), "s"(lds_base) : "memory");
}
__device__ __forceinline__ u32x4 ldg16(const void* p) { return *(const GAS u32x4*)p; }
__device__ __forceinline__ u32x2 tr_read(const LAS unsigned char* p) {
  return __builtin_bit_cast(u32x2, __builtin_amdgcn_ds_read_tr16_b64_v4i16((LAS s16x4*)p));
}
__device__ __forceinline__ int fresh_lane() {
  int l;
  asm volatile("v_mbcnt_lo_u32_b32 %0, -1, 0\n\tv_mbcnt_hi_u32_b32 %0, -1, %0" : "=v"(l));
  return l;
}
#define LD16(voff, soff) __builtin_amdgcn_raw_buffer_load_b128(rs, voff, soff, AUX_SC1)
#define LDF4(voff, soff) __builtin_bit_cast(f32x4, LD16(voff, soff))
#define LDMOD(voff, soff) ({ const f32x4 t_ = LDF4(voff, WS_MODP + (soff)) + LDF4(voff, WS_MODP + 8192 + (soff)); \
    const unsigned a_ = pack2(t_[0], t_[1]), b_ = pack2(t_[2], t_[3]); (f32x4){bflo(a_), bfhi(a_), bflo(b_), bfhi(b_)}; })
#define ST16(v, voff, soff) do { const u32x4 d_ = (v); __builtin_amdgcn_raw_buffer_store_b128(d_, rs, voff, soff, AUX_SC1); asm volatile("s_nop 1" :: "v"(d_)); } while (0)
__device__ __forceinline__ unsigned umax4(u32x4 d) { return max(max(d[0], d[1]), max(d[2], d[3])); }

__device__ __forceinline__ void ln_stats(const u32x4 (&xr)[8], float (&rs)[8], float (&nb)[8]) {
  float s[8], q[8];
#pragma unroll
  for (int i = 0; i < 8; ++i) {
    s[i] = dot2bf(xr[i][0], 0x3f803f80u, dot2bf(xr[i][1], 0x3f803f80u, dot2bf(xr[i][2], 0x3f803f80u, dot2bf(xr[i][3], 0x3f803f80u, 0.f))));
    q[i] = dot2bf(xr[i][0], xr[i][0], dot2bf(xr[i][1], xr[i][1], dot2bf(xr[i][2], xr[i][2], dot2bf(xr[i][3], xr[i][3], 0.f))));
  }
  float ts[4], tq[4];
#pragma unroll
  for (int i = 0; i < 4; ++i) { ts[i] = fold32(s[i], s[i + 4]); tq[i] = fold32(q[i], q[i + 4]); }
#pragma unroll
  for (int i = 0; i < 2; ++i) {
    const float mu = row16_sum(fold16(ts[i], ts[i + 2])) * (1.f / D_);
    const float e2 = row16_sum(fold16(tq[i], tq[i + 2])) * (1.f / D_);
    const float r = __builtin_amdgcn_rsqf(fmaxf(fmaf(-mu, mu, e2), 0.f) + LN_EPS);
    const float n = -mu * r;
#pragma unroll
    for (int p = 0; p < 4; ++p) { rs[i + 2 * p] = rdlane(r, 16 * p); nb[i + 2 * p] = rdlane(n, 16 * p); }
  }
}

#define SUB4(v, i) __builtin_shufflevector(v, v, 4 * (i), 4 * (i) + 1, 4 * (i) + 2, 4 * (i) + 3)
__device__ __forceinline__ unsigned lds_arrive(LAS unsigned char* p, int lane) {
  asm volatile("s_waitcnt lgkmcnt(0)" ::: "memory");
  unsigned old = 0;
  if (lane == 0) old = __hip_atomic_fetch_add((LAS unsigned*)p, 1u, __ATOMIC_RELAXED, __HIP_MEMORY_SCOPE_WORKGROUP);
  return __builtin_amdgcn_readfirstlane(old);
}
template <int N> __device__ __forceinline__ void lds_wait(LAS unsigned char* p) {
  unsigned v, t, n;
  asm volatile("s_mov_b32 %2, 0x20000\n"
               "1:\nds_read_b32 %0, %3\ns_waitcnt lgkmcnt(0)\nv_readfirstlane_b32 %1, %0\ns_cmp_ge_u32 %1, %4\ns_cbranch_scc1 2f\n"
               "s_sleep 1\ns_sub_u32 %2, %2, 1\ns_cmp_lg_u32 %2, 0\ns_cbranch_scc1 1b\n2:\n"
               : "=&v"(v), "=&s"(t), "=&s"(n) : "v"((unsigned)(uintptr_t)p), "i"(N) : "memory", "scc");
}
__device__ __forceinline__ void lds_arrive_flag(LAS unsigned char* p, unsigned char* flag, unsigned voff, unsigned val, unsigned mask) {
  unsigned v, t; u64 e;
  asm volatile("s_mov_b64 %2, exec\ns_mov_b64 exec, 1\nds_add_rtn_u32 %0, %3, %4\ns_waitcnt lgkmcnt(0)\nv_readfirstlane_b32 %1, %0\n"
               "s_cmp_lg_u32 %1, 7\ns_cbranch_scc1 1f\ns_mov_b32 exec_lo, %8\nglobal_store_dword %6, %7, %5 sc1\n1:\ns_mov_b64 exec, %2\n"
               : "=&v"(v), "=&s"(t), "=&s"(e) : "v"((unsigned)(uintptr_t)p), "v"(1u), "s"(flag), "v"(voff), "v"(val), "s"(mask) : "memory", "scc");
}
template <int CTRL> __device__ __forceinline__ unsigned dppu(unsigned v) { return __builtin_amdgcn_update_dpp(0u, v, CTRL, 0xf, 0xf, true); }
__device__ __forceinline__ unsigned wave_umax(unsigned t) {
  t = max(t, dppu<0xB1>(t)); t = max(t, dppu<0x4E>(t)); t = max(t, dppu<0x141>(t)); t = max(t, dppu<0x140>(t));
  auto r = __builtin_amdgcn_permlane16_swap(t, t, false, false);
  t = max(r[0], r[1]);
  auto r2 = __builtin_amdgcn_permlane32_swap(t, t, false, false);
  return max(r2[0], r2[1]);
}
__device__ __forceinline__ float knorm2(u32x4 k) { return dot2bf(k[0], k[0], dot2bf(k[1], k[1], dot2bf(k[2], k[2], dot2bf(k[3], k[3], 0.f)))); }

__device__ __forceinline__ void wait_flags(const unsigned char* ws, int kind, int first, int n, int lane) {
  const GAS unsigned* f = (const GAS unsigned*)(ws + WS_FLAG + (kind * 256 + first + (lane < n ? lane : 0)) * 128);
#pragma unroll 1
  for (int spins = 0; spins < SPIN_CAP; ++spins) {
    const unsigned v = __hip_atomic_load(f, RLX_AGENT);
    if (__all(v == 1u)) break;
    __builtin_amdgcn_s_sleep(1);
  }
  __builtin_amdgcn_fence(__ATOMIC_ACQUIRE, "wavefront");
}
#define PINS(x) asm volatile("" : "+s"(x))
#define DRAIN_BAR() do { SBAR(); asm volatile("s_waitcnt vmcnt(0) lgkmcnt(0)\n\ts_barrier" ::: "memory"); SBAR(); } while (0)
__device__ __forceinline__ void raise_flag(unsigned char* flag, int w) {
  unsigned a, b; u64 e;
  asm volatile("s_cmp_lg_u32 %3, 0\ns_cbranch_scc1 1f\ns_mov_b64 %2, exec\ns_mov_b64 exec, 1\nv_mov_b32 %0, 0\nv_mov_b32 %1, 1\n"
               "global_store_dword %0, %1, %4 sc1\ns_mov_b64 exec, %2\n1:\n" : "=&v"(a), "=&v"(b), "=&s"(e) : "s"(w), "s"(flag) : "memory", "scc");
}

#define ATT_TILE_A(KO, V1O, V0O, ADV, X1, X2) \
  "s_waitcnt lgkmcnt(0)\nv_mfma_f32_32x32x16_bf16 v[208:223], v[228:231], v[224:227], v[160:175]\n" \
  "ds_read_b128 v[228:231], v248 offset:" KO "\n" \
  "v_exp_f32 v192, v192\nv_exp_f32 v193, v193\nv_exp_f32 v194, v194\nv_cvt_pk_bf16_f32 v240, v192, v193\n" \
  X1 \
  "v_exp_f32 v195, v195\nv_exp_f32 v196, v196\nv_cvt_pk_bf16_f32 v241, v194, v195\nv_exp_f32 v197, v197\n" \
  "v_mfma_f32_32x32x16_bf16 v[176:191], v[236:239], v[244:247], v[176:191]\n" \
  "ds_read_b128 v[236:239], v249 offset:" V1O "\n" \
  ADV \
  "v_exp_f32 v198, v198\nv_cvt_pk_bf16_f32 v242, v196, v197\nv_exp_f32 v199, v199\nv_exp_f32 v200, v200\n" \
  "v_cvt_pk_bf16_f32 v243, v198, v199\nv_exp_f32 v201, v201\nv_exp_f32 v202, v202\nv_cvt_pk_bf16_f32 v244, v200, v201\n" \
  "v_mfma_f32_32x32x16_bf16 v[176:191], v[232:235], v[240:243], v[176:191]\n" \
  "ds_read_b128 v[232:235], v249 offset:" V0O "\n" \
  "v_exp_f32 v203, v203\nv_exp_f32 v204, v204\nv_exp_f32 v205, v205\nv_cvt_pk_bf16_f32 v245, v202, v203\n" \
  X2 \
  "v_exp_f32 v206, v206\nv_exp_f32 v207, v207\nv_cvt_pk_bf16_f32 v246, v204, v205\nv_cvt_pk_bf16_f32 v247, v206, v207\n"
#define ATT_TILE_B(KO, V1O, V0O, ADV, X1, X2) \
  "s_waitcnt lgkmcnt(0)\nv_mfma_f32_32x32x16_bf16 v[192:207], v[228:231], v[224:227], v[160:175]\n" \
  "ds_read_b128 v[228:231], v248 offset:" KO "\n" \
  "v_exp_f32 v208, v208\nv_exp_f32 v209, v209\nv_exp_f32 v210, v210\nv_cvt_pk_bf16_f32 v240, v208, v209\n" \
  X1 \
  "v_exp_f32 v211, v211\nv_exp_f32 v212, v212\nv_cvt_pk_bf16_f32 v241, v210, v211\nv_exp_f32 v213, v213\n" \
  "v_mfma_f32_32x32x16_bf16 v[176:191], v[236:239], v[244:247], v[176:191]\n" \
  "ds_read_b128 v[236:239], v249 offset:" V1O "\n" \
  ADV \
  "v_exp_f32 v214, v214\nv_cvt_pk_bf16_f32 v242, v212, v213\nv_exp_f32 v215, v215\nv_exp_f32 v216, v216\n" \
  "v_cvt_pk_bf16_f32 v243, v214, v215\nv_exp_f32 v217, v217\nv_exp_f32 v218, v218\nv_cvt_pk_bf16_f32 v244, v216, v217\n" \
  "v_mfma_f32_32x32x16_bf16 v[176:191], v[232:235], v[240:243], v[176:191]\n" \
  "ds_read_b128 v[232:235], v249 offset:" V0O "\n" \
  "v_exp_f32 v219, v219\nv_exp_f32 v220, v220\nv_exp_f32 v221, v221\nv_cvt_pk_bf16_f32 v245, v218, v219\n" \
  X2 \
  "v_exp_f32 v222, v222\nv_exp_f32 v223, v223\nv_cvt_pk_bf16_f32 v246, v220, v221\nv_cvt_pk_bf16_f32 v247, v222, v223\n"
#define ATT_ASM(a0, b0, a1, b1, a2, b2, a3, b3, a4, b4, a5, b5, a6, b6, a7, b7) \
  "s_nop 1\n" \
  ATT_TILE_A("2048", "512", "1024", "", a0, b0) \
  ATT_TILE_B("3072", "1536", "2048", "", a1, b1) \
  ATT_TILE_A("4096", "2560", "3072", "", a2, b2) \
  ATT_TILE_B("5120", "3584", "4096", "", a3, b3) \
  ATT_TILE_A("6144", "4608", "5120", "", a4, b4) \
  ATT_TILE_B("7168", "5632", "6144", "", a5, b5) \
  ATT_TILE_A("16384", "6656", "7168", "", a6, b6) \
  ATT_TILE_B("17408", "7680", "0", "v_add_u32 v249, v249, v250\nv_add_u32 v248, 0x4000, v248\n", a7, b7) \
  "s_waitcnt lgkmcnt(0)\ns_nop 3\n"

#define ATT_STATE "+{v[176:191]}"(o), "+{v[192:207]}"(sA), "+{v[228:231]}"(kf), "+{v[232:235]}"(v0), "+{v[236:239]}"(v1), \
                  "+{v[244:247]}"(p1), "+{v248}"(kp), "+{v249}"(vp)
#define ATT_IN "{v[160:175]}"(cin), "{v[224:227]}"(qf), "{v250}"(vstep)
#define ATT_CLOB "v208", "v209", "v210", "v211", "v212", "v213", "v214", "v215", "v216", "v217", "v218", "v219", "v220", "v221", "v222", \
                 "v223", "v240", "v241", "v242", "v243", "memory"
#define ATT_PLAIN() do { SBAR(); asm volatile(ATT_ASM("", "", "", "", "", "", "", "", "", "", "", "", "", "", "", "") \
    : ATT_STATE : ATT_IN : ATT_CLOB); SBAR(); } while (0)
#define GL(R, VO, OFF) "global_load_dwordx4 " R ", " VO ", %[sb] offset:" OFF " sc1\n"
#define DMA(VO, OFF) "global_load_lds_dwordx4 " VO ", %[sb] offset:" OFF " sc1\n"

__global__ void __launch_bounds__(512, 2)
fused_block(const u16* x, const u16* wq, const u16* wk, const u16* wv,
            const u16* __restrict__ wo, const u16* __restrict__ w1, const u16* __restrict__ w2, const u16* cvec,
            const u16* wmod, unsigned char* ws, u16* __restrict__ out) {
  __shared__ __attribute__((aligned(16))) unsigned char lds[L_END];
  LAS unsigned char* L = (LAS unsigned char*)lds;
  const unsigned lds0 = (unsigned)(uintptr_t)lds;
  const int bid = blockIdx.x, tid = threadIdx.x, lane = tid & 63, w = __builtin_amdgcn_readfirstlane(tid >> 6);
  const int rb = (bid & 7) * 4 + ((bid >> 3) & 3), m = bid >> 5;
  const int g = lane >> 4, c = lane & 15, hi = lane >> 5, c32 = lane & 31;
  const __amdgpu_buffer_rsrc_t rs = __builtin_amdgcn_make_buffer_rsrc(ws, 0, WS_END, 0x00020000);
  STAMP(0);
  const unsigned t_start = (unsigned)__builtin_amdgcn_s_memrealtime() & 0x7fffffffu;

  const bool crit = bid < 64;
  if (crit) {
    if (w == 0 && lane < 32) ST16(((u32x4){SENT, SENT, SENT, SENT}), lane * 16, WS_KF + bid * 512);
    if (w == 2) ST16(((u32x4){SENT, SENT, SENT, SENT}), lane * 16, WS_KV + bid * 1024);
    if (w == 1 && lane < 12) __builtin_amdgcn_raw_buffer_store_b32(0u, rs, lane * 128, WS_FLAG + bid * 1536, AUX_SC1);
    CFENCE();
  }
  if (tid < 8) *(LAS unsigned*)(L + L_CNT + tid * 4) = 0u;
  const u64 epoch = *(const __attribute__((address_space(4))) u64*)(ws + WS_EPOCH) + 0x9E3779B97F4A7C15ull;
  const bool late = (bid >> 6) == 1, kvwg = bid >= 128;
  const unsigned l16 = lane * 16, l16b = lane * 16 + 8192;
#define BL16(D, R, V, S) asm volatile("buffer_load_dwordx4 %0, %1, %2, %3 offen" : "=&v"(D) : "v"(V), "s"(R), "s"(S) : "memory")
#define BL2(D, R, V, S) asm volatile("buffer_load_ushort %0, %1, %2, %3 offen" : "=&v"(D) : "v"(V), "s"(R), "s"(S) : "memory")
  u32x4 wm[4];
  unsigned cv[4];
  const unsigned mvo = (tid >> 3) * 6144 + (tid & 7) * 16, cvo = (tid >> 3) * 2;
  const __amdgpu_buffer_rsrc_t xs = __builtin_amdgcn_make_buffer_rsrc((void*)x, 0, 2048 * 512 * 2, 0x00020000);
#define XROW(i) (16 * ((((i) >> 1) + m) & 3) + 2 * w + ((i) & 1))
  u32x4 xr[8];
#define SILU(v) ((v) * __builtin_amdgcn_rcpf(1.f + __builtin_amdgcn_exp2f((v) * -1.4426950408889634f)))
#define MOD_PART(N) do { \
    float p[8] = {0.f, 0.f, 0.f, 0.f, 0.f, 0.f, 0.f, 0.f}; \
    _Pragma("unroll") for (int i = 0; i < N; ++i) { const unsigned sl = pack2(SILU(bflo(cv[i])), 0.f), sh_ = sl << 16; \
      _Pragma("unroll") for (int q = 0; q < 4; ++q) { p[2 * q] = dot2bf(wm[i][q], sl, p[2 * q]); p[2 * q + 1] = dot2bf(wm[i][q], sh_, p[2 * q + 1]); } } \
    float t0 = fold16(fold32(p[0], p[4]), fold32(p[2], p[6])), t1 = fold16(fold32(p[1], p[5]), fold32(p[3], p[7])); \
    t0 += dpp<0x128>(t0); t1 += dpp<0x128>(t1); \
    if (c < 8) *(LAS f32x2*)(L + L_RED + (w * 64 + c * 8 + 2 * g) * 4) = (f32x2){t0, t1}; } while (0)
#define MOD_SUM() ({ float s_ = 0.f; _Pragma("unroll") for (int i = 0; i < 8; ++i) s_ += *(const LAS float*)(L + L_RED + (i * 64 + lane) * 4); s_; })
  {
    const __amdgpu_buffer_rsrc_t ms = __builtin_amdgcn_make_buffer_rsrc((void*)wmod, 0, crit ? 512 * 3072 * 2 : 0, 0x00020000);
    const __amdgpu_buffer_rsrc_t cs = __builtin_amdgcn_make_buffer_rsrc((void*)cvec, 0, crit ? 512 * 2 : 0, 0x00020000);
    const int so = (bid >> 4) * 128 * 6144 + (bid & 15) * 128;
    BL16(wm[0], ms, mvo, so); BL16(wm[1], ms, mvo, so + 64 * 6144);
    BL2(cv[0], cs, cvo, (bid >> 4) * 256); BL2(cv[1], cs, cvo, (bid >> 4) * 256 + 128);
#pragma unroll
    for (int i = 0; i < 4; ++i) BL16(xr[i], xs, l16, (64 * rb + XROW(i)) * 1024);
    asm volatile("s_waitcnt vmcnt(4)" : "+v"(wm[0]), "+v"(wm[1]), "+v"(cv[0]), "+v"(cv[1]) :: "memory");
  }
  if (crit) {
    MOD_PART(2);
    __syncthreads();
    const float s = MOD_SUM();
    const u64 gr = (epoch & ~0xffffffull) | ((__float_as_uint(s) + 0x80u) >> 8);
    GAS u64* gp = (GAS u64*)(ws + WS_MODG + w * 32768 + (bid >> 4) * 8192 + ((bid & 15) * 64 + lane) * 8);
    __hip_atomic_store(gp, gr, RLX_AGENT);
    __hip_atomic_store(gp + 32768, gr, RLX_AGENT);
  }
  STAMP(1);
#pragma unroll
  for (int i = 4; i < 8; ++i) BL16(xr[i], xs, l16, (64 * rb + XROW(i)) * 1024);
  {
    u64 pc; unsigned clen;
    asm volatile("s_getpc_b64 %0\n.Lfb_code_lo:\ns_mov_b32 %1, .Lfb_code_hi-.Lfb_code_lo" : "=s"(pc), "=s"(clen));
    const __amdgpu_buffer_rsrc_t a0 = __builtin_amdgcn_make_buffer_rsrc(late ? (void*)wmod : (kvwg ? (void*)wk : (void*)pc), 0, late ? 512 * 3072 * 2 : (kvwg ? 16384 : clen), 0x00020000);
    const __amdgpu_buffer_rsrc_t a1 = __builtin_amdgcn_make_buffer_rsrc(late ? (void*)wmod : (void*)wv, 0, late ? 512 * 3072 * 2 : (kvwg ? 16384 : 0), 0x00020000);
    const __amdgpu_buffer_rsrc_t cs = __builtin_amdgcn_make_buffer_rsrc((void*)cvec, 0, late ? 512 * 2 : 0, 0x00020000);
    const unsigned vo = late ? mvo : w * 2048 + l16;
    const int s0 = late ? (m & 1) * 256 * 6144 + 2048 + (bid & 31) * 128 : (kvwg ? 0 : (bid >> 3) * 16384), sd = late ? 64 * 6144 : 1024, s2 = late ? s0 + 128 * 6144 : 0;
    BL16(wm[0], a0, vo, s0); BL16(wm[1], a0, vo, s0 + sd); BL16(wm[2], a1, vo, s2); BL16(wm[3], a1, vo, s2 + sd);
#pragma unroll
    for (int i = 0; i < 4; ++i) BL2(cv[i], cs, cvo, (m & 1) * 512 + i * 128);
  }
  asm volatile("s_waitcnt vmcnt(8)" : "+v"(xr[0]), "+v"(xr[1]), "+v"(xr[2]), "+v"(xr[3]), "+v"(xr[4]), "+v"(xr[5]), "+v"(xr[6]), "+v"(xr[7]) :: "memory");
  float rsd[8], nb[8];
  ln_stats(xr, rsd, nb);
  const int hh = w >> 1, rt = w & 1;
  asm volatile("s_waitcnt vmcnt(0)" : "+v"(wm[0]), "+v"(wm[1]), "+v"(wm[2]), "+v"(wm[3]), "+v"(cv[0]), "+v"(cv[1]), "+v"(cv[2]), "+v"(cv[3]) :: "memory");
  u32x4 kfr[4];
  {
    const unsigned ov = (c & 11) == 0 ? 0x3f803f80u : 0u;
    *(LAS u32x4*)(L + L_ONES + tid * 16) = (u32x4){ov, ov, ov, ov};
    if (kvwg) {
#pragma unroll
      for (int i = 0; i < 4; ++i) *(LAS u32x4*)(L + L_H1 + w * 4096 + i * 1024 + lane * 16) = wm[i];
      const LAS unsigned char* b0 = L + L_H1 + w * 4096 + (8 * g + (c >> 2)) * 32 + (c & 3) * 8;
      asm volatile("s_waitcnt lgkmcnt(0)" ::: "memory");
#pragma unroll
      for (int i = 0; i < 4; ++i) {
        const u32x2 lo = tr_read(b0 + i * 1024), hi2 = tr_read(b0 + i * 1024 + 128);
        kfr[i] = (u32x4){lo[0], lo[1], hi2[0], hi2[1]};
      }
    }
  }
  const u16* gsrc; int gld, gup, gbytes, gdst, gdd; unsigned gvo, gvo2;
  {
    const int pi = bid * 8 + w;
    int rbase, col, dcol;
    if (pi < 256) { const int mm = pi >> 5, ks = (pi >> 1) & 15, tp = pi & 1;
      gsrc = wo; gld = 1024; gup = 5; dcol = 4; gbytes = 512 * 512 * 2; gdst = WS_WO + pi * 2048; gdd = 1024;
      rbase = 16 * (2 * ks + (g >> 1)) + 4 * (g & 1); col = 64 * mm + 32 * tp + 8 * (c >> 2) + (c & 3); }
    else if (pi < 1152) { const int id = pi - 256, a = id >> 5, ks = id & 31;
      gsrc = w1; gld = 2 * F_; gup = 1; dcol = 32; gbytes = 512 * F_ * 2; gdst = WS_W1 + (a * 64 + ks) * 1024; gdd = 32768;
      rbase = 16 * ks + 8 * hi; col = 64 * a + c32; }
    else { const int id = pi - 1152, mm = id / 112, ks = id - mm * 112;
      const int kk = ks % 14 - ((7 * mm) >> 2);
      gsrc = w2; gld = 1024; gup = 5; dcol = 32; gbytes = 512 * F_ * 2; gdst = WS_W2 + (id - ks % 14 + (kk < 0 ? kk + 14 : kk)) * 2048; gdd = 1024;
      rbase = 16 * ks + 4 * hi; col = 64 * mm + 16 * ((c32 >> 2) & 1) + (c32 & 3) + 4 * (c32 >> 3); }
    gvo = rbase * gld + col * 2; gvo2 = gvo + dcol * 2; gup *= gld;
  }
  constexpr int NXH = 2;
  f32x2 xh[NXH][4];
#define XHAT(i, q) (__builtin_elementwise_fma((f32x2){bflo(xr[i][q]), bfhi(xr[i][q])}, (f32x2){rsd[i], rsd[i]}, (f32x2){nb[i], nb[i]}))
#pragma unroll
  for (int i = 0; i < NXH; ++i) {
#pragma unroll
    for (int q = 0; q < 4; ++q) xh[i][q] = XHAT(i, q);
    asm volatile("" : "+v"(xh[i][0]), "+v"(xh[i][1]), "+v"(xh[i][2]), "+v"(xh[i][3]));
  }
  u32x4 mg[4];
  asm volatile("" : "+s"(rsd[0]), "+s"(rsd[1]), "+s"(nb[6]), "+s"(nb[7]) :: "memory");
  SBAR();
#define BL16C(D, V, S) asm volatile("buffer_load_dwordx4 %0, %1, %2, %3 offen sc1" : "=&v"(D) : "v"(V), "s"(rs), "s"(S) : "memory")
#pragma unroll
  for (int i = 0; i < 4; ++i) BL16C(mg[i], tid * 16, WS_MODG + (bid & 15) * 32768 + i * 8192);
  {
    const __amdgpu_buffer_rsrc_t qs = __builtin_amdgcn_make_buffer_rsrc((void*)wq, 0, 512 * 512 * 2, 0x00020000);
    const int r0 = tid >> 3, f = ((r0 >> 1) & 1) | ((r0 >> 2) & 2);
    const unsigned qvo = r0 * 1024 + m * 128 + (((tid & 7) ^ (f << 1)) << 4);
#pragma unroll
    for (int i = 0; i < 8; ++i) {
      const int t = (i + 2 * (bid >> 3)) & 7;
      asm volatile("s_mov_b32 m0, %3\n\ts_nop 0\n\tbuffer_load_dwordx4 %0, %1, %2 offen lds" :: "v"(qvo), "s"(qs), "s"(t * 65536), "s"(lds0 + t * 8192 + w * 1024) : "memory");
    }
  }
  asm volatile("s_waitcnt vmcnt(8)" : "+v"(mg[0]), "+v"(mg[1]), "+v"(mg[2]), "+v"(mg[3]) :: "memory");
  STAMP(2);
  {
    const unsigned thi = (unsigned)(epoch >> 32), tlo = (unsigned)epoch;
#pragma unroll 1
    for (int spins = 0; spins < SPIN_CAP; ++spins) {
      unsigned bad = 0u;
#pragma unroll
      for (int i = 0; i < 4; ++i) bad |= (mg[i][1] ^ thi) | (mg[i][3] ^ thi) | (((mg[i][0] ^ tlo) | (mg[i][2] ^ tlo)) & 0xff000000u);
      if (__all(bad == 0u)) break;
      if (spins == 0) COUNT(5);
      __builtin_amdgcn_s_sleep(2);
      CFENCE();
#pragma unroll
      for (int i = 0; i < 4; ++i) mg[i] = LD16(tid * 16, WS_MODG + (bid & 15) * 32768 + i * 8192);
    }
    const float a0 = (__uint_as_float(mg[0][0] << 8) + __uint_as_float(mg[1][0] << 8)) + (__uint_as_float(mg[2][0] << 8) + __uint_as_float(mg[3][0] << 8));
    const float a1 = (__uint_as_float(mg[0][2] << 8) + __uint_as_float(mg[1][2] << 8)) + (__uint_as_float(mg[2][2] << 8) + __uint_as_float(mg[3][2] << 8));
    unsigned t = pack2(a0, a1);
    if (w >= 4) t = pack2(1.f + bflo(t), 1.f + bfhi(t));
    *(LAS unsigned*)(L + L_MODA + tid * 4) = t;
  }
  __syncthreads();
  STAMP(3);
  u32x4 wf[16];
  u32x16 ge;
  {
    const u32x4 sa = *(const LAS u32x4*)(L + L_MODA + lane * 16), ca = *(const LAS u32x4*)(L + L_MODA + 1024 + lane * 16);
    f32x2 sc[4], sh[4];
#pragma unroll
    for (int q = 0; q < 4; ++q) { sc[q] = (f32x2){bflo(ca[q]), bfhi(ca[q])}; sh[q] = (f32x2){bflo(sa[q]), bfhi(sa[q])}; }
#define H_ROW(i) do { u32x4 o_; \
      _Pragma("unroll") for (int q = 0; q < 4; ++q) { const f32x2 t_ = __builtin_elementwise_fma((i) < NXH ? xh[(i) < NXH ? (i) : 0][q] : XHAT(i, q), sc[q], sh[q]); o_[q] = pack2(t_[0], t_[1]); } \
      *(LAS u32x4*)(L + L_H1 + XROW(i) * HROW + lane * 16) = o_; } while (0)
    H_ROW(0); H_ROW(1);
    unsigned kn = 0u;
    if (kvwg) {
      __syncthreads();
      const int sub = m - 4;
      const LAS unsigned char* hp = L + L_H1 + (16 * sub + c) * HROW + g * 16 + w * 128;
      const u32x4 h0 = *(const LAS u32x4*)hp, h1 = *(const LAS u32x4*)(hp + 64);
      f32x4 ak = {0.f, 0.f, 0.f, 0.f}, av = ak;
      ak = mfma16(kfr[0], h0, ak); av = mfma16(h0, kfr[2], av);
      ak = mfma16(kfr[1], h1, ak); av = mfma16(h1, kfr[3], av);
      *(LAS f32x4*)(L + L_WKV + w * 2048 + lane * 16) = ak;
      *(LAS f32x4*)(L + L_WKV + w * 2048 + 1024 + lane * 16) = av;
      __syncthreads();
      if (w < 2) {
        ak = *(const LAS f32x4*)(L + L_WKV + w * 1024 + lane * 16);
#pragma unroll
        for (int i = 1; i < 8; ++i) ak += *(const LAS f32x4*)(L + L_WKV + i * 2048 + w * 1024 + lane * 16);
        const u32x2 ok = {min(pack2(ak[0], ak[1]), SENT - 1u), min(pack2(ak[2], ak[3]), SENT - 1u)};
        const int tile = 2 * rb + (sub >> 1), s = sub & 1;
        const int kvo = WS_KV + (tile >> 3) * 16384 + (tile & 7) * 1024 + (w ? 8192 + s * 512 : s * 256);
        __builtin_amdgcn_raw_buffer_store_b64(ok, rs, w ? (g & 1) * 256 + c * 16 + (g >> 1) * 8 : ((g >> 1) * 32 + c) * 16 + (g & 1) * 8, kvo, AUX_SC1);
        STAMP(7);
        if (w == 0) {
          float n2 = dot2bf(ok[0], ok[0], dot2bf(ok[1], ok[1], 0.f));
          n2 = fold32(n2, n2);
          n2 = fold16(n2, n2);
          kn = min(wave_umax(__float_as_uint(n2)), SENT - 1u);
        }
      }
    }
    {
      const __amdgpu_buffer_rsrc_t gs = __builtin_amdgcn_make_buffer_rsrc((void*)gsrc, 0, gbytes, 0x00020000);
      unsigned so = 0;
#define GA(A, B, STEP) "buffer_load_ushort v" #A ", %[g0], %[gs], %[so] offen\nbuffer_load_ushort v" #B ", %[g1], %[gs], %[so] offen\ns_add_u32 %[so], %[so], " STEP "\n"
      asm volatile(GA(32, 40, "%[gl]") GA(33, 41, "%[gl]") GA(34, 42, "%[gl]") GA(35, 43, "%[gu]") GA(36, 44, "%[gl]") GA(37, 45, "%[gl]") GA(38, 46, "%[gl]") GA(39, 47, "%[gl]")
                   : "=&{v[32:47]}"(ge), [so] "+s"(so) : [g0] "v"(gvo), [g1] "v"(gvo2), [gs] "s"(gs), [gl] "s"(gld), [gu] "s"(gup) : "memory", "scc");
    }
    H_ROW(2); H_ROW(3); H_ROW(4); H_ROW(5); H_ROW(6); H_ROW(7);
    if (late) MOD_PART(4);
    asm volatile("s_waitcnt vmcnt(16)" ::: "memory");
    if (kvwg && w < 2 && lane < 16) __builtin_amdgcn_raw_buffer_store_b32(kn, rs, lane * 1024, WS_KF + ((bid - 128) * 2 + w) * 4, AUX_SC1);
    __syncthreads();
    STAMP(4);
    if (late && w == 0)
      __builtin_amdgcn_raw_buffer_store_b32(__float_as_uint(MOD_SUM()), rs, lane * 4, WS_MODP + (m & 1) * 8192 + (bid & 31) * 256, AUX_SC1);
  }
  const int kvw = (w >> 1) * 16384 + (w & 1) * 4096;
  u32x4 qf, kvs[8];
  {
    f32x4 acc0 = {0.f, 0.f, 0.f, 0.f}, acc1 = acc0;
    const LAS unsigned char* hp = L + L_H1 + (32 * rt + c) * HROW + g * 16;
    const int q4 = c >> 2, p4 = c & 3;
    const LAS unsigned char* a0 = L + (8 * g + q4) * 128 + ((hh ^ (((q4 >> 1) & 1) | ((g & 1) << 1))) << 5) + p4 * 8;
    u32x4 b0[16], b1[16];
#define Q_RD(ks) do { const u32x2 lo = tr_read(a0 + (ks) * 4096), hi2 = tr_read(a0 + (ks) * 4096 + 512); wf[ks] = (u32x4){lo[0], lo[1], hi2[0], hi2[1]}; \
      b0[ks] = *(const LAS u32x4*)(hp + (ks) * 64); b1[ks] = *(const LAS u32x4*)(hp + 16 * HROW + (ks) * 64); } while (0)
    Q_RD(0); Q_RD(1); Q_RD(2);
    SBAR();
#pragma unroll
    for (int ks = 0; ks < 16; ++ks) {
      if (ks >= KV0 && ks < KV0 + 8) kvs[ks - KV0] = LD16(l16, WS_KV + kvw + ((ks - KV0) >> 2) * 8192 + ((ks - KV0) & 3) * 1024);
      if (ks + 3 < 16) Q_RD(ks + 3);
      acc0 = mfma16(wf[ks], b0[ks], acc0);
      acc1 = mfma16(wf[ks], b1[ks], acc1);
      SBAR();
    }
    auto r0 = __builtin_amdgcn_permlane16_swap(pack2(acc0[0] * QSCALE, acc0[1] * QSCALE), pack2(acc1[0] * QSCALE, acc1[1] * QSCALE), false, false);
    auto r1 = __builtin_amdgcn_permlane16_swap(pack2(acc0[2] * QSCALE, acc0[3] * QSCALE), pack2(acc1[2] * QSCALE, acc1[3] * QSCALE), false, false);
    qf = (u32x4){r0[0], r1[0], r0[1], r1[1]};
  }
  STAMP(5);
  float q2 = knorm2(qf);
  q2 = fold32(q2, q2);
  __syncthreads();
  {
    unsigned mx = 0u;
#pragma unroll
    for (int i = 0; i < 8; ++i) mx = max(mx, umax4(kvs[i]));
    if (__builtin_expect(__any(mx == SENT), 0)) {
#pragma unroll 1
      for (int spins = 0; spins < SPIN_CAP; ++spins) {
        unsigned bad = 0u;
#pragma unroll
        for (int i = 0; i < 8; ++i) bad |= __any(umax4(kvs[i]) == SENT) ? 1u << i : 0u;
        if (!bad) break;
        if (spins == 0) COUNT(3);
        __builtin_amdgcn_s_sleep(1);
        CFENCE();
#pragma unroll
        for (int i = 0; i < 8; ++i)
          if (bad >> i & 1u) kvs[i] = LD16(l16, WS_KV + kvw + (i >> 2) * 8192 + (i & 3) * 1024);
      }
    }
#pragma unroll
    for (int i = 0; i < 8; ++i) *(LAS u32x4*)(L + kvw + (i >> 2) * 8192 + (i & 3) * 1024 + lane * 16) = kvs[i];
    unsigned km = max(__float_as_uint(fold32(knorm2(kvs[0]), knorm2(kvs[1]))), __float_as_uint(fold32(knorm2(kvs[2]), knorm2(kvs[3]))));
    km = max(km, dppu<0xB1>(km)); km = max(km, dppu<0x4E>(km)); km = max(km, dppu<0x141>(km)); km = max(km, dppu<0x140>(km));
    if (c == 0) asm volatile("ds_max_u32 %0, %1" :: "v"(lds0 + L_CNT + 28), "v"(km) : "memory");
  }
  f32x16 o, cin;
#pragma unroll
  for (int r = 0; r < 16; ++r) o[r] = 0.f;
  unsigned kp = lds0 + lane * 16;
  unsigned vp = lds0 + ((c32 < 16) ? (8192 + hi * 256 + c32 * 16) : (L_ONES + (c32 - 16) * 16));
  unsigned vstep = (c32 < 16) ? 16384 : 0;
  u32x4 v1 = {0u, 0u, 0u, 0u}, p1 = {0u, 0u, 0u, 0u};
  asm volatile("" : "+{v[176:191]}"(o), "+{v[236:239]}"(v1), "+{v[244:247]}"(p1), "+{v248}"(kp), "+{v249}"(vp), "+{v250}"(vstep));
  u32x4 kfl;
  asm volatile("s_waitcnt lgkmcnt(0)\n\ts_barrier" ::: "memory");
  const float boundA = __builtin_amdgcn_sqrtf(q2 * *(const LAS float*)(L + L_CNT + 28)) * 1.0001f;
#pragma unroll
  for (int r = 0; r < 16; ++r) cin[r] = -boundA;
  STAMP(6);
  f32x16 sA;
  u32x4 kf, v0;
  asm volatile("ds_read_b128 v[228:231], v248\nds_read_b128 v[232:235], v249\ns_waitcnt lgkmcnt(1)\n"
               "v_mfma_f32_32x32x16_bf16 v[192:207], v[228:231], v[224:227], v[160:175]\nds_read_b128 v[228:231], v248 offset:1024\n"
               "s_nop 7\ns_nop 2\ns_waitcnt lgkmcnt(0)\n"
               : "=&{v[192:207]}"(sA), "=&{v[228:231]}"(kf), "=&{v[232:235]}"(v0) : "{v[160:175]}"(cin), "{v[224:227]}"(qf), "{v248}"(kp), "{v249}"(vp) : "memory");
  __builtin_amdgcn_s_setprio(PRIO0);
  const int nt = 7 * m + (w < 7 ? w : 0);
  {
#define PK(D, LO, HI) "v_lshl_or_b32 v" #D ", v" #HI ", 16, v" #LO "\n"
#define GS(R, B, OFF) "buffer_store_dwordx4 " R ", %[va], %[rs], " B " offen offset:" OFF " sc1\n"
#define GF(B, OFF) GS("v[48:51]", B, OFF)
#define SENT4 "v_mov_b32 v48, -1\nv_mov_b32 v49, -1\nv_mov_b32 v50, -1\nv_mov_b32 v51, -1\n"
    SBAR();
    asm volatile(SENT4
                 ATT_ASM("", "", "", "", "buffer_load_dwordx4 v[52:55], %[va], %[rs], %[kf] offen sc1\n", "",
                         PK(32, 32, 33) PK(33, 34, 35) PK(34, 36, 37) PK(35, 38, 39) GS("v[32:35]", "%[d0]", "0"), "", "", "",
                         PK(40, 40, 41) PK(41, 42, 43) PK(42, 44, 45) PK(43, 46, 47) GS("v[40:43]", "%[d1]", "0"), "", "", "",
                         GF("%[fo]", "0"), "")
                 "s_waitcnt vmcnt(3)\n"
                 : ATT_STATE, "+{v[32:47]}"(ge), "=&{v[52:55]}"(kfl)
                 : ATT_IN, [va] "v"(l16), [rs] "s"(rs), [kf] "s"(WS_KF + (bid & 15) * 1024), [d0] "s"(gdst), [d1] "s"(gdst + gdd),
                   [fo] "s"(WS_OB + ((rb * 2 + rt) * 32 + 4 * m + hh) * 1024)
                 : ATT_CLOB, "v48", "v49", "v50", "v51");
    SBAR();
  }
#pragma unroll 1
  for (int spins = 0; spins < SPIN_CAP && __any(umax4(kfl) == SENT); ++spins) {
    if (spins == 0) COUNT(4);
    __builtin_amdgcn_s_sleep(4);
    CFENCE();
    kfl = LD16(l16, WS_KF + (bid & 15) * 1024);
  }
  const unsigned kmB = umax4(kfl);
  STAMP(22);
  __builtin_amdgcn_s_setprio(PRIO1);
  {
    SBAR();
    asm volatile("s_mov_b32 m0, %[ld]\n"
                 ATT_ASM(DMA("%[va]", "0"), DMA("%[va]", "1024"), DMA("%[va]", "2048"), DMA("%[va]", "3072") "s_add_u32 m0, m0, 0x2000\n",
                         DMA("%[vb]", "0"), DMA("%[vb]", "1024"), DMA("%[vb]", "2048"), DMA("%[vb]", "3072"), "", "", "", "", "", "", "", "") "s_waitcnt vmcnt(0)\n"
                 : ATT_STATE
                 : ATT_IN, [va] "v"(l16), [vb] "v"(l16b), [sb] "s"(ws + WS_KV + 65536 + kvw), [ld] "s"(lds0 + 65536 + kvw) : ATT_CLOB, "scc");
    SBAR();
  }
  lds_arrive_flag(L + L_CNT, ws + WS_AUXF + bid * 4, l16 * 64, t_start, 0xffffu);
  STAMP(23);
  __builtin_amdgcn_s_setprio(PRIO2);
#define ATT_FILL(F0, F1, X) do { SBAR(); \
    asm volatile(SENT4 ATT_ASM(GF("%[f0]", "0"), "", "", "", "", "", GF("%[f1]", "0"), "", "", "", "", "", X, "", "", "") \
                 : ATT_STATE : ATT_IN, [va] "v"(l16), [rs] "s"(rs), [f0] "s"(F0), [f1] "s"(F1) : ATT_CLOB, "v48", "v49", "v50", "v51"); SBAR(); } while (0)
  ATT_FILL(WS_YB + ((rb * 2 + (w >> 2)) * 32 + 4 * m + (w & 3)) * 1024, WS_HB + (rb * 224 + 112 + 2 * nt) * 1024, GF("%[f1]", "1024"));
  lds_wait<8>(L + L_CNT);
  {
    const float boundB = fmaxf(__builtin_amdgcn_sqrtf(q2 * __uint_as_float(wave_umax(kmB))) * 1.0001f, boundA);
    const float dl = boundA - boundB, f = __builtin_amdgcn_exp2f(dl);
    o = mfma32(v1, p1, o);
    p1 = (u32x4){0u, 0u, 0u, 0u};
#pragma unroll
    for (int r = 0; r < 16; ++r) { sA[r] += dl; cin[r] = -boundB; }
#pragma unroll
    for (int r = 0; r < 9; ++r) o[r] *= f;
  }
  STAMP(24);
  __builtin_amdgcn_s_setprio(PRIO3);
  ATT_FILL(WS_HB + (rb * 224 + 2 * nt) * 1024, w < 7 ? WS_HB + (rb * 224 + 2 * nt + 1) * 1024 : WS_ST + rb * 8192 + m * 1024, "");
  lds_arrive(L + L_CNT + 8, lane);
  CFENCE();
  u32x4 af = LD16(l16, WS_AUXF + (bid & 15) * 1024);
  STAMP(25);
  __builtin_amdgcn_s_setprio(PRIO4);
  ATT_PLAIN();
#pragma unroll 1
  for (int spins = 0; spins < SPIN_CAP && __any(umax4(af) == SENT); ++spins) {
    if (spins == 0) COUNT(6);
    __builtin_amdgcn_s_sleep(8);
    CFENCE();
    af = LD16(l16, WS_AUXF + (bid & 15) * 1024);
  }
  unsigned lead = __builtin_amdgcn_readfirstlane(wave_umax(max(max((t_start - af[0] + 100000u) & 0x7fffffffu, (t_start - af[1] + 100000u) & 0x7fffffffu), max((t_start - af[2] + 100000u) & 0x7fffffffu, (t_start - af[3] + 100000u) & 0x7fffffffu)))) - 100000u;
  if (bid == 0 && tid == 0) __hip_atomic_store((GAS u64*)(ws + WS_EPOCH), epoch, RLX_AGENT);
  f32x4 tb0, tb1;
  asm volatile("global_load_dwordx4 %0, %2, %4 sc1\nglobal_load_dwordx4 %1, %3, %4 sc1" : "=&v"(tb0), "=&v"(tb1) : "v"(l16), "v"(l16b), "s"(ws + WS_MODP + w * 1024) : "memory");
  u32x16 w1t[8];
  unsigned char* wpre = ws + WS_W1 + nt * 32768;
  u32x4 xin;
  asm volatile("global_load_dwordx4 %0, %1, %2" : "=&v"(xin) : "v"(c * 1024 + g * 16), "s"(x + (size_t)(64 * rb + 16 * (w & 3)) * D_ + 64 * m + 32 * (w >> 2)) : "memory");
#define ATT_W1(B, T0, T1, T2, T3) do { SBAR(); \
    asm volatile(ATT_ASM(GL("v[" #B "+0:" #B "+3]", "%[va]", "-4096"), GL("v[" #B "+4:" #B "+7]", "%[va]", "-3072"), \
                         GL("v[" #B "+8:" #B "+11]", "%[va]", "-2048"), GL("v[" #B "+12:" #B "+15]", "%[va]", "-1024"), \
                         GL("v[" #B "+16:" #B "+19]", "%[va]", "0"), GL("v[" #B "+20:" #B "+23]", "%[va]", "1024"), \
                         GL("v[" #B "+24:" #B "+27]", "%[va]", "2048"), GL("v[" #B "+28:" #B "+31]", "%[va]", "3072"), \
                         GL("v[" #B "+32:" #B "+35]", "%[vb]", "-4096"), GL("v[" #B "+36:" #B "+39]", "%[vb]", "-3072"), \
                         GL("v[" #B "+40:" #B "+43]", "%[vb]", "-2048"), GL("v[" #B "+44:" #B "+47]", "%[vb]", "-1024"), \
                         GL("v[" #B "+48:" #B "+51]", "%[vb]", "0"), GL("v[" #B "+52:" #B "+55]", "%[vb]", "1024"), \
                         GL("v[" #B "+56:" #B "+59]", "%[vb]", "2048"), GL("v[" #B "+60:" #B "+63]", "%[vb]", "3072")) \
                 : ATT_STATE, T0, T1, T2, T3 \
                 : ATT_IN, [va] "v"(l16), [vb] "v"(l16b), [sb] "s"(wpre + ((B) - 32) * 256 + 4096) : ATT_CLOB); SBAR(); } while (0)
  STAMP(26);
  __builtin_amdgcn_s_setprio(PRIO5);
  ATT_W1(32, "=&{v[32:47]}"(w1t[0]), "=&{v[48:63]}"(w1t[1]), "=&{v[64:79]}"(w1t[2]), "=&{v[80:95]}"(w1t[3]));
  asm volatile("s_waitcnt vmcnt(16)" : "+v"(tb0), "+v"(tb1), "+v"(xin) :: "memory");
  {
    const f32x4 t = tb0 + tb1;
    u32x2 tp = {pack2(t[0], t[1]), pack2(t[2], t[3])};
    if ((w >> 1) == 2) tp = (u32x2){pack2(1.f + bflo(tp[0]), 1.f + bfhi(tp[0])), pack2(1.f + bflo(tp[1]), 1.f + bfhi(tp[1]))};
    *(LAS u32x2*)(L + L_TAB + w * 512 + (l16 >> 1)) = tp;
  }
  lds_arrive(L + L_CNT + 24, lane);
  STAMP(27);
  __builtin_amdgcn_s_setprio(PRIO6);
  ATT_W1(96, "=&{v[96:111]}"(w1t[4]), "=&{v[112:127]}"(w1t[5]), "=&{v[128:143]}"(w1t[6]), "=&{v[144:159]}"(w1t[7]));
  lds_wait<8>(L + L_CNT + 8);
  STAMP(28);
  __builtin_amdgcn_s_setprio(PRIO7);
  {
    unsigned sv;
    SBAR();
    asm volatile("s_mov_b32 %[sv], m0\ns_mov_b32 m0, %[ld]\n"
                 ATT_ASM(DMA("%[va]", "0"), DMA("%[va]", "1024"), DMA("%[va]", "2048"), DMA("%[va]", "3072") "s_add_u32 m0, m0, 0x2000\n",
                         DMA("%[vb]", "0"), DMA("%[vb]", "1024"), DMA("%[vb]", "2048"), DMA("%[vb]", "3072"), "", "", "", "", "", "", "", "")
                 "s_mov_b32 m0, %[sv]\n"
                 : ATT_STATE, [sv] "=&s"(sv)
                 : ATT_IN, [va] "v"(l16), [vb] "v"(l16b), [sb] "s"(ws + WS_WO + m * 65536 + kvw), [ld] "s"(lds0 + kvw) : ATT_CLOB, "scc");
    SBAR();
  }
  __builtin_amdgcn_s_setprio(0);
  STAMP(29);
  DRAIN();
  u32x4 w1f[32];
  asm volatile("" : "={v[32:35]}"(w1f[0]), "={v[36:39]}"(w1f[1]), "={v[40:43]}"(w1f[2]), "={v[44:47]}"(w1f[3]) : "{v[32:47]}"(w1t[0]));
  asm volatile("" : "={v[48:51]}"(w1f[4]), "={v[52:55]}"(w1f[5]), "={v[56:59]}"(w1f[6]), "={v[60:63]}"(w1f[7]) : "{v[48:63]}"(w1t[1]));
  asm volatile("" : "={v[64:67]}"(w1f[8]), "={v[68:71]}"(w1f[9]), "={v[72:75]}"(w1f[10]), "={v[76:79]}"(w1f[11]) : "{v[64:79]}"(w1t[2]));
  asm volatile("" : "={v[80:83]}"(w1f[12]), "={v[84:87]}"(w1f[13]), "={v[88:91]}"(w1f[14]), "={v[92:95]}"(w1f[15]) : "{v[80:95]}"(w1t[3]));
  asm volatile("" : "={v[96:99]}"(w1f[16]), "={v[100:103]}"(w1f[17]), "={v[104:107]}"(w1f[18]), "={v[108:111]}"(w1f[19]) : "{v[96:111]}"(w1t[4]));
  asm volatile("" : "={v[112:115]}"(w1f[20]), "={v[116:119]}"(w1f[21]), "={v[120:123]}"(w1f[22]), "={v[124:127]}"(w1f[23]) : "{v[112:127]}"(w1t[5]));
  asm volatile("" : "={v[128:131]}"(w1f[24]), "={v[132:135]}"(w1f[25]), "={v[136:139]}"(w1f[26]), "={v[140:143]}"(w1f[27]) : "{v[128:143]}"(w1t[6]));
  asm volatile("" : "={v[144:147]}"(w1f[28]), "={v[148:151]}"(w1f[29]), "={v[152:155]}"(w1f[30]), "={v[156:159]}"(w1f[31]) : "{v[144:159]}"(w1t[7]));
  o = mfma32(v1, p1, o);
  {
  const int lane = fresh_lane(), tid = w * 64 + lane;
  const int g = lane >> 4, c = lane & 15, hi = lane >> 5, c32 = lane & 31;
  if (!__all(o[8] >= 5.421010862427522e-20f)) {
    f32x16 z;
#pragma unroll
    for (int r = 0; r < 16; ++r) { z[r] = 0.f; o[r] = 0.f; }
    float mrun = -INFINITY;
    const unsigned ov = (c32 >= 16 && (c & 11) == 0) ? 0x3f803f80u : 0u;
#pragma unroll 1
    for (int t = 0; t < 64; ++t) {
      const int tb = WS_KV + (t >> 3) * 16384 + (t & 7) * 1024;
      const u32x4 kk = LD16(lane * 16, tb);
      u32x4 va = LD16(hi * 256 + c * 16, tb + 8192);
      u32x4 vb = LD16(hi * 256 + c * 16, tb + 8192 + 512);
      if (c32 >= 16) { va = (u32x4){ov, ov, ov, ov}; vb = va; }
      const f32x16 s = mfma32(kk, qf, z);
      float tm = s[0];
#pragma unroll
      for (int r = 1; r < 16; ++r) tm = fmaxf(tm, s[r]);
      const float mn = fmaxf(mrun, max32(tm));
      const float al = __builtin_amdgcn_exp2f(mrun - mn);
      mrun = mn;
      u32x4 q0, q1;
#pragma unroll
      for (int q = 0; q < 4; ++q) {
        q0[q] = pack2(__builtin_amdgcn_exp2f(s[2 * q] - mn), __builtin_amdgcn_exp2f(s[2 * q + 1] - mn));
        q1[q] = pack2(__builtin_amdgcn_exp2f(s[8 + 2 * q] - mn), __builtin_amdgcn_exp2f(s[8 + 2 * q + 1] - mn));
      }
#pragma unroll
      for (int r = 0; r < 16; ++r) o[r] *= al;
      o = mfma32(va, q0, o);
      o = mfma32(vb, q1, o);
    }
  }
  STAMP(9);
#define CANON(v) do { _Pragma("unroll") for (int q_ = 0; q_ < 4; ++q_) v[q_] = min(v[q_], SENT - 1u); } while (0)
  {
    const float rl = __builtin_amdgcn_rcpf(o[8]);
    u32x4 of;
#pragma unroll
    for (int q = 0; q < 4; ++q) of[q] = pack2(o[2 * q] * rl, o[2 * q + 1] * rl);
    CANON(of);
    ST16(of, lane * 16, WS_OB + ((rb * 2 + rt) * 32 + 4 * m + hh) * 1024);
  }
#define W1FRAG(ks) w1f[ks]
  f32x16 uv;
#pragma unroll
  for (int r = 0; r < 16; ++r) uv[r] = 0.f;
  const LAS unsigned char* tp = L + L_TAB + 2048 - (c32 & 1) * 1024 + hi * 16;
#define UV_PART(K0, K1) do { u32x4 tf_[K1 - K0]; SBAR(); \
    _Pragma("unroll") for (int ks = K0; ks < K1; ++ks) tf_[ks - K0] = *(const LAS u32x4*)(tp + ks * 32); \
    SBAR(); \
    _Pragma("unroll") for (int ks = K0; ks < K1; ++ks) uv = mfma32(W1FRAG(ks), tf_[ks - K0], uv); \
    PIN(uv); SBAR(); } while (0)
  const bool early = lds_arrive(L + L_CNT + 16, lane) < 4u;
  lds_wait<8>(L + L_CNT + 24);
  UV_PART(0, 16);
  if (early) UV_PART(16, 32);
  const int rs4 = w & 3, kh = w >> 2;
  const int ecol = 64 * m + 32 * kh + 8 * g;
#define W2OFF(v, i) (WS_W2 + (m * 112 + 14 * (v)) * 2048 + (i) * 1024)
  u32x4 x1p;
  {
    u32x4 bf[8];
    unsigned char* obp = ws + WS_OB + ((rb * 2 + (rs4 >> 1)) * 32 + 16 * kh) * 1024 + (rs4 & 1) * 256 + 4096;
    unsigned char* fl = ws + WS_FLAG + (rb * 8 + m) * 128;
    unsigned va = g * 512 + c * 16, vb = va + 8192;
    PINS(obp); PINS(fl); PIN(va); PIN(vb);
#define O_LOAD() asm volatile(GL("%0", "%[va]", "-4096") GL("%1", "%[va]", "-2048") GL("%2", "%[va]", "0") GL("%3", "%[va]", "2048") \
                              GL("%4", "%[vb]", "-4096") GL("%5", "%[vb]", "-2048") GL("%6", "%[vb]", "0") GL("%7", "%[vb]", "2048") \
      : "=&v"(bf[0]), "=&v"(bf[1]), "=&v"(bf[2]), "=&v"(bf[3]), "=&v"(bf[4]), "=&v"(bf[5]), "=&v"(bf[6]), "=&v"(bf[7]) \
      : [va] "v"(va), [vb] "v"(vb), [sb] "s"(obp) : "memory")
    f32x4 acc[4];
    unsigned wpo = kh * 32768 + lane * 16;
    PIN(wpo);
#define WO_RD(n) (*(const LAS u32x4*)(L + wpo + (n) * 1024))
    DRAIN_BAR();
    STAMP(10);
    raise_flag(fl, w);
#pragma unroll 1
    for (int pass = 0; pass < 64; ++pass) {
      O_LOAD();
      SBAR();
#pragma unroll
      for (int t = 0; t < 4; ++t) acc[t] = (f32x4){0.f, 0.f, 0.f, 0.f};
      u32x4 wq4[32];
      unsigned mx = 0u;
#pragma unroll
      for (int n = 0; n < WRD; ++n) wq4[n] = WO_RD(n);
      SBAR();
#pragma unroll
      for (int n = 0; n < 32; ++n) {
        if (n + WRD < 32) wq4[n + WRD] = WO_RD(n + WRD);
        if ((n & 3) == 0) asm volatile("s_waitcnt vmcnt(%1)" : "+v"(bf[n >> 2]) : "i"(7 - n / 4) : "memory");
        acc[n & 3] = mfma16(wq4[n], bf[n >> 2], acc[n & 3]);
        if ((n & 3) == 1) mx = max(mx, umax4(bf[n >> 2]));
        SBAR();
      }
      if (__builtin_expect(!__any(mx == SENT), 1)) break;
      COUNT(0);
      wait_flags(ws, 0, rb * 8 + 4 * kh, 4, lane);
    }
    STAMP(11);
    LAS f32x4* ex = (LAS f32x4*)(L + 65536);
    ex[(w * 2 + 0) * 64 + lane] = kh ? acc[0] : acc[2];
    ex[(w * 2 + 1) * 64 + lane] = kh ? acc[1] : acc[3];
    const u32x4 ga = *(const LAS u32x4*)(L + L_TAB + ecol * 2), cb = *(const LAS u32x4*)(L + L_TAB + 2048 + ecol * 2);
    __syncthreads();
    const f32x4 r0 = ex[((w ^ 4) * 2 + 0) * 64 + lane], r1 = ex[((w ^ 4) * 2 + 1) * 64 + lane];
    const f32x4 a0 = (kh ? acc[2] : acc[0]) + r0, a1 = (kh ? acc[3] : acc[1]) + r1;
    x1p[0] = pack2(fmaf(bflo(ga[0]), a0[0], bflo(xin[0])), fmaf(bfhi(ga[0]), a0[1], bfhi(xin[0])));
    x1p[1] = pack2(fmaf(bflo(ga[1]), a0[2], bflo(xin[1])), fmaf(bfhi(ga[1]), a0[3], bfhi(xin[1])));
    x1p[2] = pack2(fmaf(bflo(ga[2]), a1[0], bflo(xin[2])), fmaf(bfhi(ga[2]), a1[1], bfhi(xin[2])));
    x1p[3] = pack2(fmaf(bflo(ga[3]), a1[2], bflo(xin[3])), fmaf(bfhi(ga[3]), a1[3], bfhi(xin[3])));
    u32x4 y;
#pragma unroll
    for (int q = 0; q < 4; ++q) y[q] = pack2(bflo(x1p[q]) * bflo(cb[q]), bfhi(x1p[q]) * bfhi(cb[q]));
    CANON(y);
    ST16(y, g * 512 + c * 16, WS_YB + ((rb * 2 + (rs4 >> 1)) * 32 + 4 * m + 2 * kh) * 1024 + (rs4 & 1) * 256);
    float st = fold32(dot2bf(x1p[0], 0x3f803f80u, dot2bf(x1p[1], 0x3f803f80u, dot2bf(x1p[2], 0x3f803f80u, dot2bf(x1p[3], 0x3f803f80u, 0.f)))), knorm2(x1p));
    st = fold16(st, st);
    if ((g & 1) == 0)
      __builtin_amdgcn_raw_buffer_store_b32(min(__float_as_uint(st), SENT - 1u), rs, c * 8 + hi * 4, WS_ST + rb * 8192 + (2 * m + kh) * 512 + rs4 * 128, AUX_SC1);
  }
  PIN(x1p);
  if (!early) UV_PART(16, 32);
  STAMP(12);

#define NW2V 19
#define RDV 6
  constexpr int NW2 = NW2V, RD = RDV;
  u32x4 w2f[28];
  int hoff[14];
  {
    const int h0 = WS_HB + (rb * 224 + 14 * w) * 1024;
    int t = h0 + ((7 * m) >> 2) * 1024;
#pragma unroll
    for (int n = 0; n < 14; ++n) { PINS(t); hoff[n] = t; t += 1024; t = t == h0 + 14336 ? h0 : t; }
  }
  {
    u32x4 yr[8], sp;
    int yo = WS_YB + rb * 65536 + w * 8192, so = WS_ST + rb * 8192 + w * 64;
    unsigned char* fl = ws + WS_FLAG + (256 + rb * 8 + m) * 128;
    unsigned va = lane * 16, vb = va + 4096, vs = (lane >> 2) * 512 + (lane & 3) * 16;
    PINS(yo); PINS(so); PINS(fl); PIN(va); PIN(vb); PIN(vs);
#define Y_LOAD() do { CFENCE(); _Pragma("unroll") for (int i = 0; i < 8; ++i) yr[i] = LD16((i < 4 ? va : vb) + (i & 3) * 1024, yo); sp = LD16(vs, so); } while (0)
    DRAIN_BAR();
    STAMP(13);
    Y_LOAD();
    raise_flag(fl, w);
    SBAR();
#pragma unroll 1
    for (int pass = 0; pass < 64; ++pass) {
      unsigned mx = 0u;
#pragma unroll
      for (int i = 0; i < 8; ++i) { *(LAS u32x4*)(L + (w * 8 + i) * 1024 + lane * 16) = yr[i]; mx = max(mx, umax4(yr[i])); }
      mx = max(mx, umax4(sp));
      if (__builtin_expect(!__any(mx == SENT), 1)) break;
      COUNT(1);
      wait_flags(ws, 1, rb * 8, 8, lane);
      Y_LOAD();
    }
    STAMP(14);
    float s1 = fold32(__uint_as_float(sp[0]), __uint_as_float(sp[2])), s2 = fold32(__uint_as_float(sp[1]), __uint_as_float(sp[3]));
    s1 = fold16(s1, s1); s2 = fold16(s2, s2);
    s1 += dpp<0x128>(s1); s2 += dpp<0x128>(s2);
    s1 += dpp<0x124>(s1); s2 += dpp<0x124>(s2);
    const float mu = s1 * (1.f / D_);
    const float rstd = __builtin_amdgcn_rsqf(fmaxf(fmaf(-mu, mu, s2 * (1.f / D_)), 0.f) + LN_EPS);
    if (c32 < 4) *(LAS f32x2*)(L + L_STAT + (8 * w + 2 * c32 + hi) * 8) = (f32x2){rstd, -mu};
    __syncthreads();
    STAMP(15);
    if (w < 7) {
      const LAS unsigned char* hp = L + lane * 16;
      const f32x2 st0 = *(const LAS f32x2*)(L + L_STAT + c32 * 8), st1 = *(const LAS f32x2*)(L + L_STAT + 256 + c32 * 8);
      f32x16 acc0, acc1;
#pragma unroll
      for (int r = 0; r < 16; ++r) { acc0[r] = 0.f; acc1[r] = 0.f; }
      u32x4 bq[64];
      u32x4 h0, h1;
      const int ho = WS_HB + (rb * 224 + 2 * nt) * 1024;
#define PRE(A, r, ST) fmaf(fmaf(dpp<0xA0>(uv[r]), ST[1], A[r]), ST[0], dpp<0xF5>(uv[r]))
#define HPK(A, q, ST) ({ const float a_ = PRE(A, 2 * (q), ST), b_ = PRE(A, 2 * (q) + 1, ST); min(pack2(SILU(a_), SILU(b_)), SENT - 1u); })
#pragma unroll
      for (int n = 0; n < RD; ++n) bq[n] = *(const LAS u32x4*)(hp + n * 1024);
      SBAR();
#pragma unroll
      for (int n = 0; n < 64; ++n) {
        if (n + RD < 64) bq[n + RD] = *(const LAS u32x4*)(hp + (n + RD) * 1024);
        if (n < 32) acc0 = mfma32(W1FRAG(n & 31), bq[n], acc0); else acc1 = mfma32(W1FRAG(n & 31), bq[n], acc1);
        if ((n & 3) == 0 && 11 + n / 4 < NW2) w2f[11 + n / 4] = LD16(lane * 16, W2OFF(w, 11 + n / 4));
        if (n >= 34 && n < 50 && (n & 1) == 0) {
          const int q = (n - 34) / 2;
          const unsigned t = HPK(acc0, q, st0);
          if (q < 4) { h0[q] = t; PIN(h0); } else { h1[q - 4] = t; PIN(h1); }
        }
        if (n == 42) ST16(h0, lane * 16, ho);
        if (n == 50) ST16(h1, lane * 16, ho + 1024);
        if (n >= 36 && n % 3 == 0 && NW2 + (n - 36) / 3 < 28) w2f[NW2 + (n - 36) / 3] = LD16(lane * 16, W2OFF(w, NW2 + (n - 36) / 3));
        SBAR();
      }
      PIN(uv);
#pragma unroll
      for (int q = 0; q < 4; ++q) h0[q] = HPK(acc1, q, st1);
      ST16(h0, lane * 16, ho + 112 * 1024);
#pragma unroll
      for (int q = 0; q < 4; ++q) h1[q] = HPK(acc1, 4 + q, st1);
      ST16(h1, lane * 16, ho + 113 * 1024);
    } else {
#pragma unroll 1
      for (int v = 0; v < 8; ++v) {
        asm volatile("s_mov_b32 m0, %[ld]\ns_nop 0\n" DMA("%[va]", "0") DMA("%[va]", "1024") DMA("%[va]", "2048") DMA("%[va]", "3072") "s_add_u32 m0, m0, 0x1000\ns_nop 0\n"
                     DMA("%[vb]", "0") DMA("%[vb]", "1024") DMA("%[vb]", "2048") DMA("%[vb]", "3072") "s_add_u32 m0, m0, 0x1000\ns_nop 0\n"
                     DMA("%[vc]", "0") DMA("%[vc]", "1024") DMA("%[vc]", "2048")
                     :: [va] "v"(lane * 16), [vb] "v"(lane * 16 + 4096), [vc] "v"(lane * 16 + 8192), [sb] "s"(ws + W2OFF(v, 0)), [ld] "s"(lds0 + L_W2 + v * 11264)
                     : "memory", "scc");
      }
#pragma unroll
      for (int i = 11; i < 28; ++i) { w2f[i] = LD16(lane * 16, W2OFF(7, i)); __builtin_amdgcn_s_sleep(1); SBAR(); }
      DRAIN();
    }
  }
  STAMP(16);

  {
    constexpr int WIN = 14;
    unsigned va = lane * 16, vb = va + 112 * 1024;
    PIN(va); PIN(vb);
    u32x4 hf[WIN];
    SBAR();
    asm volatile("s_waitcnt lgkmcnt(0)\n\ts_barrier" ::: "memory");
    SBAR();
#pragma unroll
    for (int n = 0; n < WIN; ++n) hf[n] = LD16(va, hoff[n]);
    SBAR();
    LAS unsigned char* rp = L + w * 16384 + c32 * 256;
    {
      unsigned pa = L_W2 + w * 11264 + va;
      PIN(pa);
#pragma unroll
      for (int i = 0; i < 11; ++i) w2f[i] = *(const LAS u32x4*)(L + pa + i * 1024);
    }
    lds_arrive(L + L_CNT + 12, lane);
#pragma unroll 1
    for (int pass = 0; pass < 64; ++pass) {
      f32x16 acc[2][2];
#pragma unroll
      for (int r = 0; r < 16; ++r) { acc[0][0][r] = 0.f; acc[0][1][r] = 0.f; acc[1][0][r] = 0.f; acc[1][1][r] = 0.f; }
      unsigned mx = 0u;
      SBAR();
#define RED_WR(R2, ct, q) *(LAS f32x4*)(rp + (R2) * 8192 + (((8 * (ct) + 4 * hi + (q)) ^ (c32 & 15)) << 4)) = \
        (f32x4){acc[R2][ct][4 * (q)], acc[R2][ct][4 * (q) + 1], acc[R2][ct][4 * (q) + 2], acc[R2][ct][4 * (q) + 3]}
#pragma unroll
      for (int n = 0; n < 28; ++n) {
        const u32x4 cur = hf[n % WIN];
        acc[n / 14][0] = mfma32(w2f[2 * (n % 14)], cur, acc[n / 14][0]);
        acc[n / 14][1] = mfma32(w2f[2 * (n % 14) + 1], cur, acc[n / 14][1]);
        mx = max(mx, umax4(cur));
        if (n < 14) hf[n] = LD16(vb, hoff[n]);
        if (n == 1) lds_arrive_flag(L + L_CNT + 20, ws + WS_FLAG + (2 * 256 + rb * 8 + m) * 128, 0u, 1u, 1u);
        if (n == 13) STAMP(17);
        if (n == 15) lds_wait<8>(L + L_CNT + 12);
        if (n >= 16 && n < 24) RED_WR(0, (n - 16) >> 2, (n - 16) & 3);
        SBAR();
      }
      STAMP(18);
#pragma unroll
      for (int i = 0; i < 8; ++i) RED_WR(1, i >> 2, i & 3);
      if (__builtin_expect(!__any(mx == SENT), 1)) break;
      COUNT(2);
      wait_flags(ws, 2, rb * 8 + w, 1, lane);
#pragma unroll
      for (int n = 0; n < WIN; ++n) hf[n] = LD16(va, hoff[n]);
    }
    STAMP(19);
    const int lane = fresh_lane(), g = lane >> 4, c = lane & 15;
    const int erow = 64 * rb + 16 * rs4 + c, ecol = 64 * m + 32 * kh + 8 * g;
    const u32x4 gb = *(const LAS u32x4*)(L + L_TAB + 3072 + ecol * 2);
    __syncthreads();
    f32x4 a0 = {0.f, 0.f, 0.f, 0.f}, a1 = a0;
#pragma unroll
    for (int p = 0; p < 8; ++p) {
      a0 += *(const LAS f32x4*)(L + p * 16384 + (16 * rs4 + c) * 256 + (((8 * kh + 2 * g) ^ c) << 4));
      a1 += *(const LAS f32x4*)(L + p * 16384 + (16 * rs4 + c) * 256 + (((8 * kh + 2 * g + 1) ^ c) << 4));
    }
    u32x4 xo;
    xo[0] = pack2(fmaf(bflo(gb[0]), a0[0], bflo(x1p[0])), fmaf(bfhi(gb[0]), a0[1], bfhi(x1p[0])));
    xo[1] = pack2(fmaf(bflo(gb[1]), a0[2], bflo(x1p[1])), fmaf(bfhi(gb[1]), a0[3], bfhi(x1p[1])));
    xo[2] = pack2(fmaf(bflo(gb[2]), a1[0], bflo(x1p[2])), fmaf(bfhi(gb[2]), a1[1], bfhi(x1p[2])));
    xo[3] = pack2(fmaf(bflo(gb[3]), a1[2], bflo(x1p[3])), fmaf(bfhi(gb[3]), a1[3], bfhi(x1p[3])));
    const __amdgpu_buffer_rsrc_t os = __builtin_amdgcn_make_buffer_rsrc(out, 0, 2048 * 512 * 2, 0x00020000);
    __builtin_amdgcn_raw_buffer_store_b128(xo, os, (erow * D_ + ecol) * 2, 0, AUX_SC1);
  }
  STAMP(20);
  if (bid == 0 && w == 0) { if (lead > 5000u) lead = 0; while (((((unsigned)__builtin_amdgcn_s_memrealtime() & 0x7fffffffu) - t_start) & 0x7fffffffu) + lead < 6000u) __builtin_amdgcn_s_sleep(1); }
  asm volatile(".Lfb_code_hi:");
  }
}

extern "C" void kernel_launch(void* const* d_in, const int* in_sizes, int n_in, void* d_out, int out_size, void* d_ws, size_t ws_size,
                              hipStream_t stream) {
  fused_block<<<256, 512, 0, stream>>>((const u16*)d_in[0], (const u16*)d_in[1], (const u16*)d_in[2], (const u16*)d_in[3],
                                       (const u16*)d_in[4], (const u16*)d_in[6], (const u16*)d_in[7], (const u16*)d_in[8],
                                       (const u16*)d_in[9], (unsigned char*)d_ws, (u16*)d_out);
}
```

```cpp
#include <hip/hip_runtime.h>
#include <hip/hip_bf16.h>
#include <stdint.h>

typedef __bf16 bf16x8 __attribute__((ext_vector_type(8)));
typedef __bf16 bf16x2 __attribute__((ext_vector_type(2)));
typedef float f32x2 __attribute__((ext_vector_type(2)));
typedef float f32x4 __attribute__((ext_vector_type(4)));
typedef float f32x16 __attribute__((ext_vector_type(16)));
typedef unsigned u32x4 __attribute__((ext_vector_type(4)));
typedef unsigned u32x2 __attribute__((ext_vector_type(2)));
typedef unsigned u32x16 __attribute__((ext_vector_type(16)));
typedef short s16x4 __attribute__((ext_vector_type(4)));
typedef unsigned short u16;
typedef unsigned long long u64;
#define LAS __attribute__((address_space(3)))
#define GAS __attribute__((address_space(1)))
#define RLX_AGENT __ATOMIC_RELAXED, __HIP_MEMORY_SCOPE_AGENT
#define SBAR() __builtin_amdgcn_sched_barrier(0)
#define PIN(x) asm volatile("" : "+v"(x))
#define DRAIN() asm volatile("s_waitcnt vmcnt(0)" ::: "memory")
#define CFENCE() asm volatile("" ::: "memory")

constexpr int D_ = 512, F_ = 1792;
constexpr float QSCALE = 0.25f * 1.4426950408889634f;
constexpr float LOG2E = 1.4426950408889634f;
constexpr float LN_EPS = 1e-5f;
constexpr int AUX_SC1 = 16;
constexpr unsigned SENT = 0xffffffffu;
constexpr unsigned long long EPOCH_STEP = 0x9E3779B97F4A7C15ull;
constexpr int SPIN_CAP = 1 << 17;

constexpr int WS_EPOCH = 0;
constexpr int WS_KF = 163840;
constexpr int WS_AUXF = 180224;
constexpr int WS_MODP = 147456;
constexpr int WS_KV = 262144;
constexpr int WS_MODG = 393216;
constexpr int WS_OB = 1310720;
constexpr int WS_ST = 917504;
constexpr int WS_YB = WS_OB + 2097152;
constexpr int WS_HB = WS_YB + 2097152;
constexpr int WS_WO = WS_HB + 7340032;
constexpr int WS_W1 = WS_WO + 512 * 1024;
constexpr int WS_W2 = WS_W1 + 1792 * 1024;
constexpr int WS_END = WS_W2 + 1792 * 1024;

constexpr int HROW = 1056;
constexpr int L_H1 = 65536;
constexpr int L_ONES = 133120;
constexpr int L_WKV = 141312;
constexpr int L_MODA = 157696;
constexpr int L_RED = 159744;
constexpr int L_CNT = 161792;
constexpr int L_END = 161920;
constexpr int L_W2 = 65536;
constexpr int L_TAB = L_MODA;
constexpr int L_ZERO = 150528;
constexpr int L_STAT = 155648;
constexpr int KV0 = 5, WRD = 6, UVS = 8;

__device__ __forceinline__ unsigned pack2(float a, float b) {
  f32x2 v = {a, b};
  return __builtin_bit_cast(unsigned, __builtin_convertvector(v, bf16x2));
}
__device__ __forceinline__ float bflo(unsigned u) { return __uint_as_float(u << 16); }
__device__ __forceinline__ float bfhi(unsigned u) { return __uint_as_float(u & 0xffff0000u); }
__device__ __forceinline__ f32x16 mfma32(u32x4 a, u32x4 b, f32x16 c) {
  return __builtin_amdgcn_mfma_f32_32x32x16_bf16(__builtin_bit_cast(bf16x8, a), __builtin_bit_cast(bf16x8, b), c, 0, 0, 0);
}
__device__ __forceinline__ f32x4 mfma16(u32x4 a, u32x4 b, f32x4 c) {
  return __builtin_amdgcn_mfma_f32_16x16x32_bf16(__builtin_bit_cast(bf16x8, a), __builtin_bit_cast(bf16x8, b), c, 0, 0, 0);
}
__device__ __forceinline__ float dot2bf(unsigned a, unsigned b, float c) {
  return __builtin_amdgcn_fdot2_f32_bf16(__builtin_bit_cast(bf16x2, a), __builtin_bit_cast(bf16x2, b), c, false);
}
template <int CTRL> __device__ __forceinline__ float dpp(float v) {
  return __int_as_float(__builtin_amdgcn_update_dpp(0, __float_as_int(v), CTRL, 0xf, 0xf, true));
}
__device__ __forceinline__ float row16_sum(float t) { t += dpp<0xB1>(t); t += dpp<0x4E>(t); t += dpp<0x141>(t); t += dpp<0x140>(t); return t; }
__device__ __forceinline__ float fold32(float a, float b) {
  auto r = __builtin_amdgcn_permlane32_swap(__float_as_uint(a), __float_as_uint(b), false, false);
  return __uint_as_float(r[0]) + __uint_as_float(r[1]);
}
__device__ __forceinline__ float fold16(float a, float b) {
  auto r = __builtin_amdgcn_permlane16_swap(__float_as_uint(a), __float_as_uint(b), false, false);
  return __uint_as_float(r[0]) + __uint_as_float(r[1]);
}
__device__ __forceinline__ float max32(float a) {
  auto r = __builtin_amdgcn_permlane32_swap(__float_as_uint(a), __float_as_uint(a), false, false);
  return fmaxf(__uint_as_float(r[0]), __uint_as_float(r[1]));
}
__device__ __forceinline__ float rdlane(float v, int l) { return __int_as_float(__builtin_amdgcn_readlane(__float_as_int(v), l)); }
__device__ __forceinline__ u32x2 tr_read(const LAS unsigned char* p) {
  return __builtin_bit_cast(u32x2, __builtin_amdgcn_ds_read_tr16_b64_v4i16((LAS s16x4*)p));
}
__device__ __forceinline__ int fresh_lane() {
  int l;
  asm volatile("v_mbcnt_lo_u32_b32 %0, -1, 0\n\tv_mbcnt_hi_u32_b32 %0, -1, %0" : "=v"(l));
  return l;
}
#define LD16(voff, soff) __builtin_amdgcn_raw_buffer_load_b128(rs, voff, soff, AUX_SC1)
#define ST16(v, voff, soff) do { const u32x4 d_ = (v); __builtin_amdgcn_raw_buffer_store_b128(d_, rs, voff, soff, AUX_SC1); asm volatile("s_nop 1" :: "v"(d_)); } while (0)
__device__ __forceinline__ unsigned umax4(u32x4 d) { return max(max(d[0], d[1]), max(d[2], d[3])); }

__device__ __forceinline__ void ln_stats(const u32x4 (&xr)[8], float (&rs)[8], float (&nb)[8]) {
  float s[8], q[8];
#pragma unroll
  for (int i = 0; i < 8; ++i) {
    s[i] = dot2bf(xr[i][0], 0x3f803f80u, dot2bf(xr[i][1], 0x3f803f80u, dot2bf(xr[i][2], 0x3f803f80u, dot2bf(xr[i][3], 0x3f803f80u, 0.f))));
    q[i] = dot2bf(xr[i][0], xr[i][0], dot2bf(xr[i][1], xr[i][1], dot2bf(xr[i][2], xr[i][2], dot2bf(xr[i][3], xr[i][3], 0.f))));
  }
  float ts[4], tq[4];
#pragma unroll
  for (int i = 0; i < 4; ++i) { ts[i] = fold32(s[i], s[i + 4]); tq[i] = fold32(q[i], q[i + 4]); }
#pragma unroll
  for (int i = 0; i < 2; ++i) {
    const float mu = row16_sum(fold16(ts[i], ts[i + 2])) * (1.f / D_);
    const float e2 = row16_sum(fold16(tq[i], tq[i + 2])) * (1.f / D_);
    const float r = __builtin_amdgcn_rsqf(fmaxf(fmaf(-mu, mu, e2), 0.f) + LN_EPS);
    const float n = -mu * r;
#pragma unroll
    for (int p = 0; p < 4; ++p) { rs[i + 2 * p] = rdlane(r, 16 * p); nb[i + 2 * p] = rdlane(n, 16 * p); }
  }
}

#define SUB4(v, i) __builtin_shufflevector(v, v, 4 * (i), 4 * (i) + 1, 4 * (i) + 2, 4 * (i) + 3)
__device__ __forceinline__ void lds_arrive(LAS unsigned char* p) {
  u64 e;
  asm volatile("s_waitcnt lgkmcnt(0)\ns_mov_b64 %0, exec\ns_mov_b64 exec, 1\nds_add_u32 %1, %2\ns_mov_b64 exec, %0\n"
               : "=&s"(e) : "v"((unsigned)(uintptr_t)p), "v"(1u) : "memory");
}
template <int N> __device__ __forceinline__ void lds_wait(LAS unsigned char* p) {
  unsigned v, t, n;
  asm volatile("s_mov_b32 %2, 0x400000\n"
               "1:\nds_read_b32 %0, %3\ns_waitcnt lgkmcnt(0)\nv_readfirstlane_b32 %1, %0\ns_cmp_ge_u32 %1, %4\ns_cbranch_scc1 2f\n"
               "s_sleep 1\ns_sub_u32 %2, %2, 1\ns_cmp_lg_u32 %2, 0\ns_cbranch_scc1 1b\n2:\n"
               : "=&v"(v), "=&s"(t), "=&s"(n) : "v"((unsigned)(uintptr_t)p), "i"(N) : "memory", "scc");
}
__device__ __forceinline__ void lds_arrive_flag(LAS unsigned char* p, unsigned char* flag, unsigned voff, unsigned val, unsigned mask) {
  unsigned v, t; u64 e;
  asm volatile("s_mov_b64 %2, exec\ns_mov_b64 exec, 1\nds_add_rtn_u32 %0, %3, %4\ns_waitcnt lgkmcnt(0)\nv_readfirstlane_b32 %1, %0\n"
               "s_cmp_lg_u32 %1, 7\ns_cbranch_scc1 1f\ns_mov_b32 exec_lo, %8\nglobal_store_dword %6, %7, %5 sc1\n1:\ns_mov_b64 exec, %2\n"
               : "=&v"(v), "=&s"(t), "=&s"(e) : "v"((unsigned)(uintptr_t)p), "v"(1u), "s"(flag), "v"(voff), "v"(val), "s"(mask) : "memory", "scc");
}
template <int CTRL> __device__ __forceinline__ unsigned dppu(unsigned v) { return __builtin_amdgcn_update_dpp(0u, v, CTRL, 0xf, 0xf, true); }
__device__ __forceinline__ unsigned wave_umax(unsigned t) {
  t = max(t, dppu<0xB1>(t)); t = max(t, dppu<0x4E>(t)); t = max(t, dppu<0x141>(t)); t = max(t, dppu<0x140>(t));
  auto r = __builtin_amdgcn_permlane16_swap(t, t, false, false);
  t = max(r[0], r[1]);
  auto r2 = __builtin_amdgcn_permlane32_swap(t, t, false, false);
  return max(r2[0], r2[1]);
}
__device__ __forceinline__ float knorm2(u32x4 k) { return dot2bf(k[0], k[0], dot2bf(k[1], k[1], dot2bf(k[2], k[2], dot2bf(k[3], k[3], 0.f)))); }

#define LDS_BAR() do { SBAR(); asm volatile("s_waitcnt lgkmcnt(0)\n\ts_barrier" ::: "memory"); SBAR(); } while (0)
#define PINS(x) asm volatile("" : "+s"(x))
#define DRAIN_BAR() do { SBAR(); asm volatile("s_waitcnt vmcnt(0) lgkmcnt(0)\n\ts_barrier" ::: "memory"); SBAR(); } while (0)

#define ATT_TILE_A(KO, V1O, V0O, ADV, X1, X2) \
  "s_waitcnt lgkmcnt(0)\nv_mfma_f32_32x32x16_bf16 v[208:223], v[228:231], v[224:227], v[160:175]\n" \
  "ds_read_b128 v[228:231], v248 offset:" KO "\n" \
  "v_exp_f32 v192, v192\nv_exp_f32 v193, v193\nv_exp_f32 v194, v194\nv_cvt_pk_bf16_f32 v240, v192, v193\n" \
  X1 \
  "v_exp_f32 v195, v195\nv_exp_f32 v196, v196\nv_cvt_pk_bf16_f32 v241, v194, v195\nv_exp_f32 v197, v197\n" \
  "v_mfma_f32_32x32x16_bf16 v[176:191], v[236:239], v[244:247], v[176:191]\n" \
  "ds_read_b128 v[236:239], v249 offset:" V1O "\n" \
  ADV \
  "v_exp_f32 v198, v198\nv_cvt_pk_bf16_f32 v242, v196, v197\nv_exp_f32 v199, v199\nv_exp_f32 v200, v200\n" \
  "v_cvt_pk_bf16_f32 v243, v198, v199\nv_exp_f32 v201, v201\nv_exp_f32 v202, v202\nv_cvt_pk_bf16_f32 v244, v200, v201\n" \
  "v_mfma_f32_32x32x16_bf16 v[176:191], v[232:235], v[240:243], v[176:191]\n" \
  "ds_read_b128 v[232:235], v249 offset:" V0O "\n" \
  "v_exp_f32 v203, v203\nv_exp_f32 v204, v204\nv_exp_f32 v205, v205\nv_cvt_pk_bf16_f32 v245, v202, v203\n" \
  X2 \
  "v_exp_f32 v206, v206\nv_exp_f32 v207, v207\nv_cvt_pk_bf16_f32 v246, v204, v205\nv_cvt_pk_bf16_f32 v247, v206, v207\n"
#define ATT_TILE_B(KO, V1O, V0O, ADV, X1, X2) \
  "s_waitcnt lgkmcnt(0)\nv_mfma_f32_32x32x16_bf16 v[192:207], v[228:231], v[224:227], v[160:175]\n" \
  "ds_read_b128 v[228:231], v248 offset:" KO "\n" \
  "v_exp_f32 v208, v208\nv_exp_f32 v209, v209\nv_exp_f32 v210, v210\nv_cvt_pk_bf16_f32 v240, v208, v209\n" \
  X1 \
  "v_exp_f32 v211, v211\nv_exp_f32 v212, v212\nv_cvt_pk_bf16_f32 v241, v210, v211\nv_exp_f32 v213, v213\n" \
  "v_mfma_f32_32x32x16_bf16 v[176:191], v[236:239], v[244:247], v[176:191]\n" \
  "ds_read_b128 v[236:239], v249 offset:" V1O "\n" \
  ADV \
  "v_exp_f32 v214, v214\nv_cvt_pk_bf16_f32 v242, v212, v213\nv_exp_f32 v215, v215\nv_exp_f32 v216, v216\n" \
  "v_cvt_pk_bf16_f32 v243, v214, v215\nv_exp_f32 v217, v217\nv_exp_f32 v218, v218\nv_cvt_pk_bf16_f32 v244, v216, v217\n" \
  "v_mfma_f32_32x32x16_bf16 v[176:191], v[232:235], v[240:243], v[176:191]\n" \
  "ds_read_b128 v[232:235], v249 offset:" V0O "\n" \
  "v_exp_f32 v219, v219\nv_exp_f32 v220, v220\nv_exp_f32 v221, v221\nv_cvt_pk_bf16_f32 v245, v218, v219\n" \
  X2 \
  "v_exp_f32 v222, v222\nv_exp_f32 v223, v223\nv_cvt_pk_bf16_f32 v246, v220, v221\nv_cvt_pk_bf16_f32 v247, v222, v223\n"
#define ATT_ASM(a0, b0, a1, b1, a2, b2, a3, b3, a4, b4, a5, b5, a6, b6, a7, b7) \
  "s_nop 1\n" \
  ATT_TILE_A("2048", "512", "1024", "", a0, b0) \
  ATT_TILE_B("3072", "1536", "2048", "", a1, b1) \
  ATT_TILE_A("4096", "2560", "3072", "", a2, b2) \
  ATT_TILE_B("5120", "3584", "4096", "", a3, b3) \
  ATT_TILE_A("6144", "4608", "5120", "", a4, b4) \
  ATT_TILE_B("7168", "5632", "6144", "", a5, b5) \
  ATT_TILE_A("16384", "6656", "7168", "", a6, b6) \
  ATT_TILE_B("17408", "7680", "0", "v_add_u32 v249, v249, v250\nv_add_u32 v248, 0x4000, v248\n", a7, b7) \
  "s_waitcnt lgkmcnt(0)\ns_nop 3\n"

#define ATT_STATE "+{v[176:191]}"(o), "+{v[192:207]}"(sA), "+{v[228:231]}"(kf), "+{v[232:235]}"(v0), "+{v[236:239]}"(v1), \
                  "+{v[244:247]}"(p1), "+{v248}"(kp), "+{v249}"(vp)
#define ATT_IN "{v[160:175]}"(cin), "{v[224:227]}"(qf), "{v250}"(vstep)
#define ATT_CLOB "v208", "v209", "v210", "v211", "v212", "v213", "v214", "v215", "v216", "v217", "v218", "v219", "v220", "v221", "v222", \
                 "v223", "v240", "v241", "v242", "v243", "memory"
#define ATT_PLAIN() do { SBAR(); asm volatile(ATT_ASM("", "", "", "", "", "", "", "", "", "", "", "", "", "", "", "") \
    : ATT_STATE : ATT_IN : ATT_CLOB); SBAR(); } while (0)
#define GL(R, VO, OFF) "global_load_dwordx4 " R ", " VO ", %[sb] offset:" OFF " sc1\n"
#define DMA(VO, OFF) "global_load_lds_dwordx4 " VO ", %[sb] offset:" OFF " sc1\n"


#define FFN_ASM \
  "v_mov_b32 v253, %[lc]\nv_mov_b32 v254, 1\ns_cmp_eq_u32 %[w], 7\ns_cbranch_scc1 70f\n" \
  "v_lshrrev_b32 v168, 1, v252\nv_and_b32 v168, 0x78, v168\nv_add_u32 v168, %[ls], v168\nds_read_b64 v[224:225], v168 offset:0\n" \
  "ds_read_b64 v[226:227], v168 offset:128\nds_read_b64 v[228:229], v168 offset:256\nds_read_b64 v[230:231], v168 offset:384\nds_read_b128 v[188:191], v252 offset:0\n" \
  "ds_read_b128 v[192:195], v252 offset:4096\nds_read_b128 v[196:199], v252 offset:8192\nds_read_b128 v[200:203], v252 offset:12288\nds_read_b128 v[204:207], v252 offset:16384\n" \
  "s_setprio 3\ns_waitcnt lgkmcnt(4)\nv_mfma_f32_16x16x32_bf16 v[24:27], v[32:35], v[188:191], 0\nv_mfma_f32_16x16x32_bf16 v[28:31], v[36:39], v[188:191], 0\n" \
  "ds_read_b128 v[188:191], v252 offset:20480\ns_waitcnt lgkmcnt(4)\nv_mfma_f32_16x16x32_bf16 v[24:27], v[40:43], v[192:195], v[24:27]\nv_mfma_f32_16x16x32_bf16 v[28:31], v[44:47], v[192:195], v[28:31]\n" \
  "ds_read_b128 v[192:195], v252 offset:24576\ns_waitcnt lgkmcnt(4)\nv_mfma_f32_16x16x32_bf16 v[24:27], v[48:51], v[196:199], v[24:27]\nv_mfma_f32_16x16x32_bf16 v[28:31], v[52:55], v[196:199], v[28:31]\n" \
  "ds_read_b128 v[196:199], v252 offset:28672\ns_waitcnt lgkmcnt(4)\nv_mfma_f32_16x16x32_bf16 v[24:27], v[56:59], v[200:203], v[24:27]\nv_mfma_f32_16x16x32_bf16 v[28:31], v[60:63], v[200:203], v[28:31]\n" \
  "ds_read_b128 v[200:203], v252 offset:32768\ns_waitcnt lgkmcnt(4)\nv_mfma_f32_16x16x32_bf16 v[24:27], v[64:67], v[204:207], v[24:27]\nv_mfma_f32_16x16x32_bf16 v[28:31], v[68:71], v[204:207], v[28:31]\n" \
  "ds_read_b128 v[204:207], v252 offset:36864\ns_waitcnt lgkmcnt(4)\nv_mfma_f32_16x16x32_bf16 v[24:27], v[72:75], v[188:191], v[24:27]\nv_mfma_f32_16x16x32_bf16 v[28:31], v[76:79], v[188:191], v[28:31]\n" \
  "ds_read_b128 v[188:191], v252 offset:40960\ns_waitcnt lgkmcnt(4)\nv_mfma_f32_16x16x32_bf16 v[24:27], v[80:83], v[192:195], v[24:27]\nv_mfma_f32_16x16x32_bf16 v[28:31], v[84:87], v[192:195], v[28:31]\n" \
  "ds_read_b128 v[192:195], v252 offset:45056\ns_waitcnt lgkmcnt(4)\nv_mfma_f32_16x16x32_bf16 v[24:27], v[88:91], v[196:199], v[24:27]\nv_mfma_f32_16x16x32_bf16 v[28:31], v[92:95], v[196:199], v[28:31]\n" \
  "ds_read_b128 v[196:199], v252 offset:49152\ns_waitcnt lgkmcnt(4)\nv_mfma_f32_16x16x32_bf16 v[24:27], v[96:99], v[200:203], v[24:27]\nv_mfma_f32_16x16x32_bf16 v[28:31], v[100:103], v[200:203], v[28:31]\n" \
  "ds_read_b128 v[200:203], v252 offset:53248\ns_waitcnt lgkmcnt(4)\nv_mfma_f32_16x16x32_bf16 v[24:27], v[104:107], v[204:207], v[24:27]\nv_mfma_f32_16x16x32_bf16 v[28:31], v[108:111], v[204:207], v[28:31]\n" \
  "ds_read_b128 v[204:207], v252 offset:57344\ns_waitcnt lgkmcnt(4)\nv_mfma_f32_16x16x32_bf16 v[24:27], v[112:115], v[188:191], v[24:27]\nv_mfma_f32_16x16x32_bf16 v[28:31], v[116:119], v[188:191], v[28:31]\n" \
  "ds_read_b128 v[188:191], v252 offset:61440\ns_waitcnt lgkmcnt(4)\nv_mfma_f32_16x16x32_bf16 v[24:27], v[120:123], v[192:195], v[24:27]\nv_mfma_f32_16x16x32_bf16 v[28:31], v[124:127], v[192:195], v[28:31]\n" \
  "ds_read_b128 v[192:195], v252 offset:1024\ns_waitcnt lgkmcnt(4)\nv_mfma_f32_16x16x32_bf16 v[24:27], v[128:131], v[196:199], v[24:27]\nv_mfma_f32_16x16x32_bf16 v[28:31], v[132:135], v[196:199], v[28:31]\n" \
  "ds_read_b128 v[196:199], v252 offset:5120\ns_waitcnt lgkmcnt(4)\nv_mfma_f32_16x16x32_bf16 v[24:27], v[136:139], v[200:203], v[24:27]\nv_mfma_f32_16x16x32_bf16 v[28:31], v[140:143], v[200:203], v[28:31]\n" \
  "ds_read_b128 v[200:203], v252 offset:9216\ns_waitcnt lgkmcnt(4)\nv_mfma_f32_16x16x32_bf16 v[24:27], v[144:147], v[204:207], v[24:27]\nv_mfma_f32_16x16x32_bf16 v[28:31], v[148:151], v[204:207], v[28:31]\n" \
  "ds_read_b128 v[204:207], v252 offset:13312\ns_waitcnt lgkmcnt(4)\nv_mfma_f32_16x16x32_bf16 v[24:27], v[152:155], v[188:191], v[24:27]\nv_mfma_f32_16x16x32_bf16 v[28:31], v[156:159], v[188:191], v[28:31]\n" \
  "ds_read_b128 v[188:191], v252 offset:17408\ns_setprio 2\ns_waitcnt lgkmcnt(4)\nv_mfma_f32_16x16x32_bf16 v[160:163], v[32:35], v[192:195], 0\n" \
  "v_mfma_f32_16x16x32_bf16 v[164:167], v[36:39], v[192:195], 0\nds_read_b128 v[192:195], v252 offset:21504\ns_waitcnt lgkmcnt(4)\nv_mfma_f32_16x16x32_bf16 v[160:163], v[40:43], v[196:199], v[160:163]\n" \
  "v_mfma_f32_16x16x32_bf16 v[164:167], v[44:47], v[196:199], v[164:167]\nds_read_b128 v[196:199], v252 offset:25600\nbuffer_load_dwordx4 v[232:235], v252, %[rs], %[w2] offen offset:0 sc1\ns_waitcnt lgkmcnt(4)\n" \
  "v_mfma_f32_16x16x32_bf16 v[160:163], v[48:51], v[200:203], v[160:163]\nv_fma_f32 v168, v208, v225, v24\nv_fma_f32 v169, v209, v225, v25\nv_fma_f32 v170, v210, v225, v26\n" \
  "v_fma_f32 v171, v211, v225, v27\nv_fma_f32 v172, v212, v225, v28\nv_mfma_f32_16x16x32_bf16 v[164:167], v[52:55], v[200:203], v[164:167]\nds_read_b128 v[200:203], v252 offset:29696\n" \
  "s_waitcnt lgkmcnt(4)\nv_mfma_f32_16x16x32_bf16 v[160:163], v[56:59], v[204:207], v[160:163]\nv_fma_f32 v173, v213, v225, v29\nv_fma_f32 v174, v214, v225, v30\n" \
  "v_fma_f32 v175, v215, v225, v31\nv_fma_f32 v168, v168, v224, v216\nv_fma_f32 v169, v169, v224, v217\nv_mfma_f32_16x16x32_bf16 v[164:167], v[60:63], v[204:207], v[164:167]\n" \
  "ds_read_b128 v[204:207], v252 offset:33792\ns_waitcnt lgkmcnt(4)\nv_mfma_f32_16x16x32_bf16 v[160:163], v[64:67], v[188:191], v[160:163]\nv_fma_f32 v170, v170, v224, v218\n" \
  "v_fma_f32 v171, v171, v224, v219\nv_exp_f32 v176, v168\nv_fma_f32 v172, v172, v224, v220\nv_mfma_f32_16x16x32_bf16 v[164:167], v[68:71], v[188:191], v[164:167]\n" \
  "ds_read_b128 v[188:191], v252 offset:37888\ns_waitcnt lgkmcnt(4)\nv_mfma_f32_16x16x32_bf16 v[160:163], v[72:75], v[192:195], v[160:163]\nv_fma_f32 v173, v173, v224, v221\n" \
  "v_fma_f32 v174, v174, v224, v222\nv_exp_f32 v177, v169\nv_fma_f32 v175, v175, v224, v223\nv_mfma_f32_16x16x32_bf16 v[164:167], v[76:79], v[192:195], v[164:167]\n" \
  "ds_read_b128 v[192:195], v252 offset:41984\ns_waitcnt lgkmcnt(4)\nv_mfma_f32_16x16x32_bf16 v[160:163], v[80:83], v[196:199], v[160:163]\nv_exp_f32 v178, v170\n" \
  "v_exp_f32 v179, v171\nv_add_f32 v176, 1.0, v176\nv_mfma_f32_16x16x32_bf16 v[164:167], v[84:87], v[196:199], v[164:167]\nds_read_b128 v[196:199], v252 offset:46080\n" \
  "s_waitcnt lgkmcnt(4)\nv_mfma_f32_16x16x32_bf16 v[160:163], v[88:91], v[200:203], v[160:163]\nv_exp_f32 v180, v172\nv_exp_f32 v181, v173\n" \
  "v_add_f32 v177, 1.0, v177\nv_mfma_f32_16x16x32_bf16 v[164:167], v[92:95], v[200:203], v[164:167]\nds_read_b128 v[200:203], v252 offset:50176\ns_waitcnt lgkmcnt(4)\n" \
  "v_mfma_f32_16x16x32_bf16 v[160:163], v[96:99], v[204:207], v[160:163]\nv_exp_f32 v182, v174\nv_exp_f32 v183, v175\nv_add_f32 v178, 1.0, v178\n" \
  "v_mfma_f32_16x16x32_bf16 v[164:167], v[100:103], v[204:207], v[164:167]\nds_read_b128 v[204:207], v252 offset:54272\ns_waitcnt lgkmcnt(4)\nv_mfma_f32_16x16x32_bf16 v[160:163], v[104:107], v[188:191], v[160:163]\n" \
  "v_rcp_f32 v176, v176\nv_add_f32 v179, 1.0, v179\nv_add_f32 v180, 1.0, v180\nv_add_f32 v181, 1.0, v181\n" \
  "v_mfma_f32_16x16x32_bf16 v[164:167], v[108:111], v[188:191], v[164:167]\nds_read_b128 v[188:191], v252 offset:58368\nbuffer_load_dwordx4 v[236:239], v252, %[rs], %[w2] offen offset:1024 sc1\ns_waitcnt lgkmcnt(4)\n" \
  "v_mfma_f32_16x16x32_bf16 v[160:163], v[112:115], v[192:195], v[160:163]\nv_rcp_f32 v177, v177\nv_add_f32 v182, 1.0, v182\nv_add_f32 v183, 1.0, v183\n" \
  "v_mul_f32 v168, v168, v176\nv_mfma_f32_16x16x32_bf16 v[164:167], v[116:119], v[192:195], v[164:167]\nds_read_b128 v[192:195], v252 offset:62464\ns_waitcnt lgkmcnt(4)\n" \
  "v_mfma_f32_16x16x32_bf16 v[160:163], v[120:123], v[196:199], v[160:163]\nv_rcp_f32 v178, v178\nv_rcp_f32 v179, v179\nv_mul_f32 v169, v169, v177\n" \
  "v_mfma_f32_16x16x32_bf16 v[164:167], v[124:127], v[196:199], v[164:167]\nds_read_b128 v[196:199], v252 offset:2048\ns_waitcnt lgkmcnt(4)\nv_mfma_f32_16x16x32_bf16 v[160:163], v[128:131], v[200:203], v[160:163]\n" \
  "v_rcp_f32 v180, v180\nv_rcp_f32 v181, v181\nv_mul_f32 v170, v170, v178\nv_mfma_f32_16x16x32_bf16 v[164:167], v[132:135], v[200:203], v[164:167]\n" \
  "ds_read_b128 v[200:203], v252 offset:6144\ns_waitcnt lgkmcnt(4)\nv_mfma_f32_16x16x32_bf16 v[160:163], v[136:139], v[204:207], v[160:163]\nv_rcp_f32 v182, v182\n" \
  "v_rcp_f32 v183, v183\nv_mul_f32 v171, v171, v179\nv_mfma_f32_16x16x32_bf16 v[164:167], v[140:143], v[204:207], v[164:167]\nds_read_b128 v[204:207], v252 offset:10240\n" \
  "s_waitcnt lgkmcnt(4)\nv_mfma_f32_16x16x32_bf16 v[160:163], v[144:147], v[188:191], v[160:163]\nv_mul_f32 v172, v172, v180\nv_mul_f32 v173, v173, v181\n" \
  "v_mul_f32 v174, v174, v182\nv_mul_f32 v175, v175, v183\nv_cvt_pk_bf16_f32 v184, v168, v169\nv_cvt_pk_bf16_f32 v185, v170, v171\n" \
  "v_mfma_f32_16x16x32_bf16 v[164:167], v[148:151], v[188:191], v[164:167]\nds_read_b128 v[188:191], v252 offset:14336\ns_waitcnt lgkmcnt(4)\nv_mfma_f32_16x16x32_bf16 v[160:163], v[152:155], v[192:195], v[160:163]\n" \
  "v_cvt_pk_bf16_f32 v186, v172, v173\nv_cvt_pk_bf16_f32 v187, v174, v175\nv_min_u32 v184, -2, v184\nv_min_u32 v185, -2, v185\n" \
  "v_min_u32 v186, -2, v186\nv_min_u32 v187, -2, v187\nbuffer_store_dwordx4 v[184:187], v252, %[rs], %[hb] offen sc1\ns_add_u32 %[hb], %[hb], 0xe000\n" \
  "v_mfma_f32_16x16x32_bf16 v[164:167], v[156:159], v[192:195], v[164:167]\nds_read_b128 v[192:195], v252 offset:18432\ns_setprio 1\ns_waitcnt lgkmcnt(4)\n" \
  "v_mfma_f32_16x16x32_bf16 v[24:27], v[32:35], v[196:199], 0\nv_mfma_f32_16x16x32_bf16 v[28:31], v[36:39], v[196:199], 0\nds_read_b128 v[196:199], v252 offset:22528\ns_waitcnt lgkmcnt(4)\n" \
  "v_mfma_f32_16x16x32_bf16 v[24:27], v[40:43], v[200:203], v[24:27]\nv_mfma_f32_16x16x32_bf16 v[28:31], v[44:47], v[200:203], v[28:31]\nds_read_b128 v[200:203], v252 offset:26624\nbuffer_load_dwordx4 v[240:243], v252, %[rs], %[w2] offen offset:2048 sc1\n" \
  "s_waitcnt lgkmcnt(4)\nv_mfma_f32_16x16x32_bf16 v[24:27], v[48:51], v[204:207], v[24:27]\nv_fma_f32 v168, v208, v227, v160\nv_fma_f32 v169, v209, v227, v161\n" \
  "v_fma_f32 v170, v210, v227, v162\nv_fma_f32 v171, v211, v227, v163\nv_fma_f32 v172, v212, v227, v164\nv_mfma_f32_16x16x32_bf16 v[28:31], v[52:55], v[204:207], v[28:31]\n" \
  "ds_read_b128 v[204:207], v252 offset:30720\ns_waitcnt lgkmcnt(4)\nv_mfma_f32_16x16x32_bf16 v[24:27], v[56:59], v[188:191], v[24:27]\nv_fma_f32 v173, v213, v227, v165\n" \
  "v_fma_f32 v174, v214, v227, v166\nv_fma_f32 v175, v215, v227, v167\nv_fma_f32 v168, v168, v226, v216\nv_fma_f32 v169, v169, v226, v217\n" \
  "v_mfma_f32_16x16x32_bf16 v[28:31], v[60:63], v[188:191], v[28:31]\nds_read_b128 v[188:191], v252 offset:34816\ns_waitcnt lgkmcnt(4)\nv_mfma_f32_16x16x32_bf16 v[24:27], v[64:67], v[192:195], v[24:27]\n" \
  "v_fma_f32 v170, v170, v226, v218\nv_fma_f32 v171, v171, v226, v219\nv_exp_f32 v176, v168\nv_fma_f32 v172, v172, v226, v220\n" \
  "v_mfma_f32_16x16x32_bf16 v[28:31], v[68:71], v[192:195], v[28:31]\nds_read_b128 v[192:195], v252 offset:38912\ns_waitcnt lgkmcnt(4)\nv_mfma_f32_16x16x32_bf16 v[24:27], v[72:75], v[196:199], v[24:27]\n" \
  "v_fma_f32 v173, v173, v226, v221\nv_fma_f32 v174, v174, v226, v222\nv_exp_f32 v177, v169\nv_fma_f32 v175, v175, v226, v223\n" \
  "v_mfma_f32_16x16x32_bf16 v[28:31], v[76:79], v[196:199], v[28:31]\nds_read_b128 v[196:199], v252 offset:43008\ns_waitcnt lgkmcnt(4)\nv_mfma_f32_16x16x32_bf16 v[24:27], v[80:83], v[200:203], v[24:27]\n" \
  "v_exp_f32 v178, v170\nv_exp_f32 v179, v171\nv_add_f32 v176, 1.0, v176\nv_mfma_f32_16x16x32_bf16 v[28:31], v[84:87], v[200:203], v[28:31]\n" \
  "ds_read_b128 v[200:203], v252 offset:47104\ns_waitcnt lgkmcnt(4)\nv_mfma_f32_16x16x32_bf16 v[24:27], v[88:91], v[204:207], v[24:27]\nv_exp_f32 v180, v172\n" \
  "v_exp_f32 v181, v173\nv_add_f32 v177, 1.0, v177\nv_mfma_f32_16x16x32_bf16 v[28:31], v[92:95], v[204:207], v[28:31]\nds_read_b128 v[204:207], v252 offset:51200\n" \
  "s_waitcnt lgkmcnt(4)\nv_mfma_f32_16x16x32_bf16 v[24:27], v[96:99], v[188:191], v[24:27]\nv_exp_f32 v182, v174\nv_exp_f32 v183, v175\n" \
  "v_add_f32 v178, 1.0, v178\nv_mfma_f32_16x16x32_bf16 v[28:31], v[100:103], v[188:191], v[28:31]\nds_read_b128 v[188:191], v252 offset:55296\ns_mov_b64 exec, 1\n" \
  "ds_add_u32 v253, v254 offset:16\ns_mov_b64 exec, -1\ns_waitcnt lgkmcnt(4)\nv_mfma_f32_16x16x32_bf16 v[24:27], v[104:107], v[192:195], v[24:27]\n" \
  "v_rcp_f32 v176, v176\nv_add_f32 v179, 1.0, v179\nv_add_f32 v180, 1.0, v180\nv_add_f32 v181, 1.0, v181\n" \
  "v_mfma_f32_16x16x32_bf16 v[28:31], v[108:111], v[192:195], v[28:31]\nds_read_b128 v[192:195], v252 offset:59392\nbuffer_load_dwordx4 v[244:247], v252, %[rs], %[w2] offen offset:3072 sc1\ns_add_u32 %[w2], %[w2], 0x1000\n" \
  "s_waitcnt lgkmcnt(4)\nv_mfma_f32_16x16x32_bf16 v[24:27], v[112:115], v[196:199], v[24:27]\nv_rcp_f32 v177, v177\nv_add_f32 v182, 1.0, v182\n" \
  "v_add_f32 v183, 1.0, v183\nv_mul_f32 v168, v168, v176\nv_mfma_f32_16x16x32_bf16 v[28:31], v[116:119], v[196:199], v[28:31]\nds_read_b128 v[196:199], v252 offset:63488\n" \
  "s_waitcnt lgkmcnt(4)\nv_mfma_f32_16x16x32_bf16 v[24:27], v[120:123], v[200:203], v[24:27]\nv_rcp_f32 v178, v178\nv_rcp_f32 v179, v179\n" \
  "v_mul_f32 v169, v169, v177\nv_mfma_f32_16x16x32_bf16 v[28:31], v[124:127], v[200:203], v[28:31]\nds_read_b128 v[200:203], v252 offset:3072\ns_waitcnt lgkmcnt(4)\n" \
  "v_mfma_f32_16x16x32_bf16 v[24:27], v[128:131], v[204:207], v[24:27]\nv_rcp_f32 v180, v180\nv_rcp_f32 v181, v181\nv_mul_f32 v170, v170, v178\n" \
  "v_mfma_f32_16x16x32_bf16 v[28:31], v[132:135], v[204:207], v[28:31]\nds_read_b128 v[204:207], v252 offset:7168\ns_waitcnt lgkmcnt(4)\nv_mfma_f32_16x16x32_bf16 v[24:27], v[136:139], v[188:191], v[24:27]\n" \
  "v_rcp_f32 v182, v182\nv_rcp_f32 v183, v183\nv_mul_f32 v171, v171, v179\nv_mfma_f32_16x16x32_bf16 v[28:31], v[140:143], v[188:191], v[28:31]\n" \
  "ds_read_b128 v[188:191], v252 offset:11264\ns_waitcnt lgkmcnt(4)\nv_mfma_f32_16x16x32_bf16 v[24:27], v[144:147], v[192:195], v[24:27]\nv_mul_f32 v172, v172, v180\n" \
  "v_mul_f32 v173, v173, v181\nv_mul_f32 v174, v174, v182\nv_mul_f32 v175, v175, v183\nv_cvt_pk_bf16_f32 v184, v168, v169\n" \
  "v_cvt_pk_bf16_f32 v185, v170, v171\nv_mfma_f32_16x16x32_bf16 v[28:31], v[148:151], v[192:195], v[28:31]\nds_read_b128 v[192:195], v252 offset:15360\ns_waitcnt lgkmcnt(4)\n" \
  "v_mfma_f32_16x16x32_bf16 v[24:27], v[152:155], v[196:199], v[24:27]\nv_cvt_pk_bf16_f32 v186, v172, v173\nv_cvt_pk_bf16_f32 v187, v174, v175\nv_min_u32 v184, -2, v184\n" \
  "v_min_u32 v185, -2, v185\nv_min_u32 v186, -2, v186\nv_min_u32 v187, -2, v187\nbuffer_store_dwordx4 v[184:187], v252, %[rs], %[hb] offen sc1\n" \
  "s_add_u32 %[hb], %[hb], 0xe000\nv_mfma_f32_16x16x32_bf16 v[28:31], v[156:159], v[196:199], v[28:31]\nds_read_b128 v[196:199], v252 offset:19456\ns_setprio 0\n" \
  "s_waitcnt lgkmcnt(4)\nv_mfma_f32_16x16x32_bf16 v[160:163], v[32:35], v[200:203], 0\nv_mfma_f32_16x16x32_bf16 v[164:167], v[36:39], v[200:203], 0\nds_read_b128 v[200:203], v252 offset:23552\n" \
  "buffer_load_dwordx4 v[32:35], v252, %[rs], %[w2] offen offset:0 sc1\ns_waitcnt lgkmcnt(4)\nv_mfma_f32_16x16x32_bf16 v[160:163], v[40:43], v[204:207], v[160:163]\nv_mfma_f32_16x16x32_bf16 v[164:167], v[44:47], v[204:207], v[164:167]\n" \
  "ds_read_b128 v[204:207], v252 offset:27648\nbuffer_load_dwordx4 v[36:39], v252, %[rs], %[w2] offen offset:1024 sc1\ns_waitcnt lgkmcnt(4)\nv_mfma_f32_16x16x32_bf16 v[160:163], v[48:51], v[188:191], v[160:163]\n" \
  "v_fma_f32 v168, v208, v229, v24\nv_fma_f32 v169, v209, v229, v25\nv_fma_f32 v170, v210, v229, v26\nv_fma_f32 v171, v211, v229, v27\n" \
  "v_fma_f32 v172, v212, v229, v28\nv_mfma_f32_16x16x32_bf16 v[164:167], v[52:55], v[188:191], v[164:167]\nds_read_b128 v[188:191], v252 offset:31744\nbuffer_load_dwordx4 v[40:43], v252, %[rs], %[w2] offen offset:2048 sc1\n" \
  "s_waitcnt lgkmcnt(4)\nv_mfma_f32_16x16x32_bf16 v[160:163], v[56:59], v[192:195], v[160:163]\nv_fma_f32 v173, v213, v229, v29\nv_fma_f32 v174, v214, v229, v30\n" \
  "v_fma_f32 v175, v215, v229, v31\nv_fma_f32 v168, v168, v228, v216\nv_fma_f32 v169, v169, v228, v217\nv_mfma_f32_16x16x32_bf16 v[164:167], v[60:63], v[192:195], v[164:167]\n" \
  "ds_read_b128 v[192:195], v252 offset:35840\nbuffer_load_dwordx4 v[44:47], v252, %[rs], %[w2] offen offset:3072 sc1\ns_add_u32 %[w2], %[w2], 0x1000\ns_waitcnt lgkmcnt(4)\n" \
  "v_mfma_f32_16x16x32_bf16 v[160:163], v[64:67], v[196:199], v[160:163]\nv_fma_f32 v170, v170, v228, v218\nv_fma_f32 v171, v171, v228, v219\nv_exp_f32 v176, v168\n" \
  "v_fma_f32 v172, v172, v228, v220\nv_mfma_f32_16x16x32_bf16 v[164:167], v[68:71], v[196:199], v[164:167]\nds_read_b128 v[196:199], v252 offset:39936\nbuffer_load_dwordx4 v[48:51], v252, %[rs], %[w2] offen offset:0 sc1\n" \
  "s_waitcnt lgkmcnt(4)\nv_mfma_f32_16x16x32_bf16 v[160:163], v[72:75], v[200:203], v[160:163]\nv_fma_f32 v173, v173, v228, v221\nv_fma_f32 v174, v174, v228, v222\n" \
  "v_exp_f32 v177, v169\nv_fma_f32 v175, v175, v228, v223\nv_mfma_f32_16x16x32_bf16 v[164:167], v[76:79], v[200:203], v[164:167]\nds_read_b128 v[200:203], v252 offset:44032\n" \
  "buffer_load_dwordx4 v[52:55], v252, %[rs], %[w2] offen offset:1024 sc1\ns_waitcnt lgkmcnt(4)\nv_mfma_f32_16x16x32_bf16 v[160:163], v[80:83], v[204:207], v[160:163]\nv_exp_f32 v178, v170\n" \
  "v_exp_f32 v179, v171\nv_add_f32 v176, 1.0, v176\nv_mfma_f32_16x16x32_bf16 v[164:167], v[84:87], v[204:207], v[164:167]\nds_read_b128 v[204:207], v252 offset:48128\n" \
  "buffer_load_dwordx4 v[56:59], v252, %[rs], %[w2] offen offset:2048 sc1\ns_waitcnt lgkmcnt(4)\nv_mfma_f32_16x16x32_bf16 v[160:163], v[88:91], v[188:191], v[160:163]\nv_exp_f32 v180, v172\n" \
  "v_exp_f32 v181, v173\nv_add_f32 v177, 1.0, v177\nv_mfma_f32_16x16x32_bf16 v[164:167], v[92:95], v[188:191], v[164:167]\nds_read_b128 v[188:191], v252 offset:52224\n" \
  "s_mov_b32 %[n], 0x400000\n77:\nds_read_b32 v255, v253 offset:16\ns_waitcnt lgkmcnt(0)\n" \
  "v_readfirstlane_b32 %[t], v255\ns_cmp_ge_u32 %[t], 7\ns_cbranch_scc1 78f\ns_sleep 1\n" \
  "s_sub_u32 %[n], %[n], 1\ns_cbranch_scc0 77b\n78:\nbuffer_load_dwordx4 v[60:63], v252, %[rs], %[h0] offen sc1\n" \
  "s_waitcnt lgkmcnt(4)\nv_mfma_f32_16x16x32_bf16 v[160:163], v[96:99], v[192:195], v[160:163]\nv_exp_f32 v182, v174\nv_exp_f32 v183, v175\n" \
  "v_add_f32 v178, 1.0, v178\nv_mfma_f32_16x16x32_bf16 v[164:167], v[100:103], v[192:195], v[164:167]\nds_read_b128 v[192:195], v252 offset:56320\nbuffer_load_dwordx4 v[64:67], v252, %[rs], %[h1] offen sc1\n" \
  "s_waitcnt lgkmcnt(4)\nv_mfma_f32_16x16x32_bf16 v[160:163], v[104:107], v[196:199], v[160:163]\nv_rcp_f32 v176, v176\nv_add_f32 v179, 1.0, v179\n" \
  "v_add_f32 v180, 1.0, v180\nv_add_f32 v181, 1.0, v181\nv_mfma_f32_16x16x32_bf16 v[164:167], v[108:111], v[196:199], v[164:167]\nds_read_b128 v[196:199], v252 offset:60416\n" \
  "buffer_load_dwordx4 v[68:71], v252, %[rs], %[h2] offen sc1\ns_waitcnt lgkmcnt(4)\nv_mfma_f32_16x16x32_bf16 v[160:163], v[112:115], v[200:203], v[160:163]\nv_rcp_f32 v177, v177\n" \
  "v_add_f32 v182, 1.0, v182\nv_add_f32 v183, 1.0, v183\nv_mul_f32 v168, v168, v176\nv_mfma_f32_16x16x32_bf16 v[164:167], v[116:119], v[200:203], v[164:167]\n" \
  "ds_read_b128 v[200:203], v252 offset:64512\nbuffer_load_dwordx4 v[72:75], v252, %[rs], %[h3] offen sc1\ns_waitcnt lgkmcnt(4)\nv_mfma_f32_16x16x32_bf16 v[160:163], v[120:123], v[204:207], v[160:163]\n" \
  "v_rcp_f32 v178, v178\nv_rcp_f32 v179, v179\nv_mul_f32 v169, v169, v177\nv_mfma_f32_16x16x32_bf16 v[164:167], v[124:127], v[204:207], v[164:167]\n" \
  "buffer_load_dwordx4 v[76:79], v252, %[rs], %[h4] offen sc1\ns_waitcnt lgkmcnt(3)\nv_mfma_f32_16x16x32_bf16 v[160:163], v[128:131], v[188:191], v[160:163]\nv_rcp_f32 v180, v180\n" \
  "v_rcp_f32 v181, v181\nv_mul_f32 v170, v170, v178\nv_mfma_f32_16x16x32_bf16 v[164:167], v[132:135], v[188:191], v[164:167]\nbuffer_load_dwordx4 v[80:83], v252, %[rs], %[h5] offen sc1\n" \
  "s_waitcnt lgkmcnt(2)\nv_mfma_f32_16x16x32_bf16 v[160:163], v[136:139], v[192:195], v[160:163]\nv_rcp_f32 v182, v182\nv_rcp_f32 v183, v183\n" \
  "v_mul_f32 v171, v171, v179\nv_mfma_f32_16x16x32_bf16 v[164:167], v[140:143], v[192:195], v[164:167]\nbuffer_load_dwordx4 v[84:87], v252, %[rs], %[h6] offen sc1\ns_waitcnt lgkmcnt(1)\n" \
  "v_mfma_f32_16x16x32_bf16 v[160:163], v[144:147], v[196:199], v[160:163]\nv_mul_f32 v172, v172, v180\nv_mul_f32 v173, v173, v181\nv_mul_f32 v174, v174, v182\n" \
  "v_mul_f32 v175, v175, v183\nv_cvt_pk_bf16_f32 v184, v168, v169\nv_cvt_pk_bf16_f32 v185, v170, v171\nv_mfma_f32_16x16x32_bf16 v[164:167], v[148:151], v[196:199], v[164:167]\n" \
  "s_waitcnt lgkmcnt(0)\nv_mfma_f32_16x16x32_bf16 v[160:163], v[152:155], v[200:203], v[160:163]\nv_cvt_pk_bf16_f32 v186, v172, v173\nv_cvt_pk_bf16_f32 v187, v174, v175\n" \
  "v_min_u32 v184, -2, v184\nv_min_u32 v185, -2, v185\nv_min_u32 v186, -2, v186\nv_min_u32 v187, -2, v187\n" \
  "buffer_store_dwordx4 v[184:187], v252, %[rs], %[hb] offen sc1\ns_add_u32 %[hb], %[hb], 0xe000\nv_mfma_f32_16x16x32_bf16 v[164:167], v[156:159], v[200:203], v[164:167]\n" \
  "s_nop 7\nv_pk_fma_f32 v[168:169], v[208:209], v[230:231], v[160:161] op_sel:[0,1,0]\nv_pk_fma_f32 v[170:171], v[210:211], v[230:231], v[162:163] op_sel:[0,1,0]\nv_pk_fma_f32 v[172:173], v[212:213], v[230:231], v[164:165] op_sel:[0,1,0]\n" \
  "v_pk_fma_f32 v[174:175], v[214:215], v[230:231], v[166:167] op_sel:[0,1,0]\nv_pk_fma_f32 v[168:169], v[168:169], v[230:231], v[216:217] op_sel_hi:[1,0,1]\nv_pk_fma_f32 v[170:171], v[170:171], v[230:231], v[218:219] op_sel_hi:[1,0,1]\nv_pk_fma_f32 v[172:173], v[172:173], v[230:231], v[220:221] op_sel_hi:[1,0,1]\n" \
  "v_pk_fma_f32 v[174:175], v[174:175], v[230:231], v[222:223] op_sel_hi:[1,0,1]\nv_exp_f32 v176, v168\nv_exp_f32 v177, v169\nv_exp_f32 v178, v170\n" \
  "v_exp_f32 v179, v171\nv_exp_f32 v180, v172\nv_exp_f32 v181, v173\nv_exp_f32 v182, v174\n" \
  "v_exp_f32 v183, v175\ns_nop 0\nv_pk_add_f32 v[176:177], v[176:177], 1.0 op_sel_hi:[1,0]\nv_pk_add_f32 v[178:179], v[178:179], 1.0 op_sel_hi:[1,0]\n" \
  "v_pk_add_f32 v[180:181], v[180:181], 1.0 op_sel_hi:[1,0]\nv_pk_add_f32 v[182:183], v[182:183], 1.0 op_sel_hi:[1,0]\nv_rcp_f32 v176, v176\nv_rcp_f32 v177, v177\n" \
  "v_rcp_f32 v178, v178\nv_rcp_f32 v179, v179\nv_rcp_f32 v180, v180\nv_rcp_f32 v181, v181\n" \
  "v_rcp_f32 v182, v182\nv_rcp_f32 v183, v183\ns_nop 0\nv_pk_mul_f32 v[168:169], v[168:169], v[176:177]\n" \
  "v_pk_mul_f32 v[170:171], v[170:171], v[178:179]\nv_pk_mul_f32 v[172:173], v[172:173], v[180:181]\nv_pk_mul_f32 v[174:175], v[174:175], v[182:183]\nv_cvt_pk_bf16_f32 v184, v168, v169\n" \
  "v_cvt_pk_bf16_f32 v185, v170, v171\nv_cvt_pk_bf16_f32 v186, v172, v173\nv_cvt_pk_bf16_f32 v187, v174, v175\nv_min_u32 v184, -2, v184\n" \
  "v_min_u32 v185, -2, v185\nv_min_u32 v186, -2, v186\nv_min_u32 v187, -2, v187\nbuffer_store_dwordx4 v[184:187], v252, %[rs], %[hb] offen sc1\n" \
  "s_add_u32 %[hb], %[hb], 0xe000\ns_branch 71f\n70:\ns_mov_b32 m0, %[pk]\n" \
  "s_mov_b32 %[n], 8\n72:\nbuffer_load_dwordx4 v252, %[rs], %[ps] offen offset:0 sc1 lds\nbuffer_load_dwordx4 v252, %[rs], %[ps] offen offset:1024 sc1 lds\n" \
  "buffer_load_dwordx4 v252, %[rs], %[ps] offen offset:2048 sc1 lds\nbuffer_load_dwordx4 v252, %[rs], %[ps] offen offset:3072 sc1 lds\ns_add_u32 m0, m0, 0x1000\ns_add_u32 %[ps], %[ps], 0x1000\n" \
  "buffer_load_dwordx4 v252, %[rs], %[ps] offen offset:0 sc1 lds\nbuffer_load_dwordx4 v252, %[rs], %[ps] offen offset:1024 sc1 lds\nbuffer_load_dwordx4 v252, %[rs], %[ps] offen offset:2048 sc1 lds\ns_add_u32 m0, m0, 0xc00\n" \
  "s_add_u32 %[ps], %[ps], 0x6000\ns_sleep 2\ns_sub_u32 %[n], %[n], 1\ns_cmp_lg_u32 %[n], 0\n" \
  "s_cbranch_scc1 72b\nbuffer_load_dwordx4 v[232:235], v252, %[rs], %[w2] offen offset:0 sc1\ns_sleep 1\nbuffer_load_dwordx4 v[236:239], v252, %[rs], %[w2] offen offset:1024 sc1\n" \
  "s_sleep 1\nbuffer_load_dwordx4 v[240:243], v252, %[rs], %[w2] offen offset:2048 sc1\ns_sleep 1\nbuffer_load_dwordx4 v[244:247], v252, %[rs], %[w2] offen offset:3072 sc1\n" \
  "s_add_u32 %[w2], %[w2], 0x1000\ns_sleep 1\nbuffer_load_dwordx4 v[32:35], v252, %[rs], %[w2] offen offset:0 sc1\ns_sleep 1\n" \
  "buffer_load_dwordx4 v[36:39], v252, %[rs], %[w2] offen offset:1024 sc1\ns_sleep 1\nbuffer_load_dwordx4 v[40:43], v252, %[rs], %[w2] offen offset:2048 sc1\ns_sleep 1\n" \
  "buffer_load_dwordx4 v[44:47], v252, %[rs], %[w2] offen offset:3072 sc1\ns_add_u32 %[w2], %[w2], 0x1000\ns_sleep 1\nbuffer_load_dwordx4 v[48:51], v252, %[rs], %[w2] offen offset:0 sc1\n" \
  "s_sleep 1\nbuffer_load_dwordx4 v[52:55], v252, %[rs], %[w2] offen offset:1024 sc1\ns_sleep 1\nbuffer_load_dwordx4 v[56:59], v252, %[rs], %[w2] offen offset:2048 sc1\n" \
  "s_sleep 1\ns_mov_b32 %[n], 0x400000\n73:\nds_read_b32 v168, v253 offset:16\n" \
  "s_waitcnt lgkmcnt(0)\nv_readfirstlane_b32 %[t], v168\ns_cmp_ge_u32 %[t], 7\ns_cbranch_scc1 74f\n" \
  "s_sleep 1\ns_sub_u32 %[n], %[n], 1\ns_cbranch_scc0 73b\n74:\n" \
  "s_sleep 8\nbuffer_load_dwordx4 v[60:63], v252, %[rs], %[h0] offen sc1\nbuffer_load_dwordx4 v[64:67], v252, %[rs], %[h1] offen sc1\nbuffer_load_dwordx4 v[68:71], v252, %[rs], %[h2] offen sc1\n" \
  "buffer_load_dwordx4 v[72:75], v252, %[rs], %[h3] offen sc1\nbuffer_load_dwordx4 v[76:79], v252, %[rs], %[h4] offen sc1\nbuffer_load_dwordx4 v[80:83], v252, %[rs], %[h5] offen sc1\nbuffer_load_dwordx4 v[84:87], v252, %[rs], %[h6] offen sc1\n" \
  "s_waitcnt vmcnt(0)\n71:\nv_mov_b32 v194, v253\nv_mov_b32 v195, 1\n" \
  "v_add_u32 v196, %[rdo], v252\nv_add_u32 v197, %[pao], v252\nv_add_u32 v198, %[pbo], v252\nv_add_u32 v253, 57344, v252\n" \
  "v_add_u32 v254, 114688, v252\nv_add_u32 v255, 172032, v252\ns_mov_b32 %[rp], 0x20000\ns_setprio 0\n" \
  "s_waitcnt lgkmcnt(0)\ns_barrier\n" \
  "ds_read_b128 v[88:91], v197 offset:0\nds_read_b128 v[92:95], v197 offset:1024\nds_read_b128 v[96:99], v197 offset:2048\nds_read_b128 v[100:103], v197 offset:3072\n" \
  "ds_read_b128 v[104:107], v198 offset:0\nds_read_b128 v[108:111], v198 offset:1024\nds_read_b128 v[112:115], v198 offset:2048\nds_read_b128 v[116:119], v198 offset:3072\n" \
  "ds_read_b128 v[120:123], v198 offset:4096\nds_read_b128 v[124:127], v198 offset:5120\nds_read_b128 v[128:131], v198 offset:6144\n80:\n" \
  "buffer_load_dwordx4 v[132:135], v253, %[rs], %[h0] offen sc1\nbuffer_load_dwordx4 v[136:139], v253, %[rs], %[h1] offen sc1\nv_mov_b32 v192, 0\ns_waitcnt vmcnt(4)\n" \
  "v_mfma_f32_16x16x32_bf16 v[160:163], v[0:3], v[60:63], 0\nbuffer_load_dwordx4 v[140:143], v253, %[rs], %[h2] offen sc1\nv_mfma_f32_16x16x32_bf16 v[164:167], v[4:7], v[60:63], 0\nv_max3_u32 v192, v192, v60, v61\n" \
  "v_mfma_f32_16x16x32_bf16 v[168:171], v[8:11], v[60:63], 0\nv_max3_u32 v192, v192, v62, v63\nv_mfma_f32_16x16x32_bf16 v[172:175], v[12:15], v[60:63], 0\nv_mfma_f32_16x16x32_bf16 v[160:163], v[16:19], v[64:67], v[160:163]\n" \
  "buffer_load_dwordx4 v[144:147], v253, %[rs], %[h3] offen sc1\nv_mfma_f32_16x16x32_bf16 v[164:167], v[20:23], v[64:67], v[164:167]\nv_max3_u32 v192, v192, v64, v65\nv_mfma_f32_16x16x32_bf16 v[168:171], v[232:235], v[64:67], v[168:171]\n" \
  "v_max3_u32 v192, v192, v66, v67\nv_mfma_f32_16x16x32_bf16 v[172:175], v[236:239], v[64:67], v[172:175]\nv_mfma_f32_16x16x32_bf16 v[160:163], v[240:243], v[68:71], v[160:163]\nbuffer_load_dwordx4 v[148:151], v253, %[rs], %[h4] offen sc1\n" \
  "v_mfma_f32_16x16x32_bf16 v[164:167], v[244:247], v[68:71], v[164:167]\nv_max3_u32 v192, v192, v68, v69\nv_mfma_f32_16x16x32_bf16 v[168:171], v[32:35], v[68:71], v[168:171]\nv_max3_u32 v192, v192, v70, v71\n" \
  "v_mfma_f32_16x16x32_bf16 v[172:175], v[36:39], v[68:71], v[172:175]\nv_mfma_f32_16x16x32_bf16 v[160:163], v[40:43], v[72:75], v[160:163]\nbuffer_load_dwordx4 v[152:155], v253, %[rs], %[h5] offen sc1\nv_mfma_f32_16x16x32_bf16 v[164:167], v[44:47], v[72:75], v[164:167]\n" \
  "v_max3_u32 v192, v192, v72, v73\nv_mfma_f32_16x16x32_bf16 v[168:171], v[48:51], v[72:75], v[168:171]\nv_max3_u32 v192, v192, v74, v75\nv_mfma_f32_16x16x32_bf16 v[172:175], v[52:55], v[72:75], v[172:175]\n" \
  "s_waitcnt lgkmcnt(0)\ns_mov_b64 exec, 1\nds_add_u32 v194, v195 offset:12\ns_mov_b64 exec, -1\n" \
  "v_mfma_f32_16x16x32_bf16 v[160:163], v[56:59], v[76:79], v[160:163]\nbuffer_load_dwordx4 v[156:159], v253, %[rs], %[h6] offen sc1\nv_mfma_f32_16x16x32_bf16 v[164:167], v[88:91], v[76:79], v[164:167]\nv_max3_u32 v192, v192, v76, v77\n" \
  "v_mfma_f32_16x16x32_bf16 v[168:171], v[92:95], v[76:79], v[168:171]\nv_max3_u32 v192, v192, v78, v79\nv_mfma_f32_16x16x32_bf16 v[172:175], v[96:99], v[76:79], v[172:175]\nv_mfma_f32_16x16x32_bf16 v[160:163], v[100:103], v[80:83], v[160:163]\n" \
  "buffer_load_dwordx4 v[60:63], v254, %[rs], %[h0] offen sc1\nv_mfma_f32_16x16x32_bf16 v[164:167], v[104:107], v[80:83], v[164:167]\nv_max3_u32 v192, v192, v80, v81\nv_mfma_f32_16x16x32_bf16 v[168:171], v[108:111], v[80:83], v[168:171]\n" \
  "v_max3_u32 v192, v192, v82, v83\nv_mfma_f32_16x16x32_bf16 v[172:175], v[112:115], v[80:83], v[172:175]\nv_mfma_f32_16x16x32_bf16 v[160:163], v[116:119], v[84:87], v[160:163]\nbuffer_load_dwordx4 v[64:67], v254, %[rs], %[h1] offen sc1\n" \
  "v_mfma_f32_16x16x32_bf16 v[164:167], v[120:123], v[84:87], v[164:167]\nv_max3_u32 v192, v192, v84, v85\nv_mfma_f32_16x16x32_bf16 v[168:171], v[124:127], v[84:87], v[168:171]\nv_max3_u32 v192, v192, v86, v87\n" \
  "v_mfma_f32_16x16x32_bf16 v[172:175], v[128:131], v[84:87], v[172:175]\ns_waitcnt vmcnt(8)\nv_mfma_f32_16x16x32_bf16 v[176:179], v[0:3], v[132:135], 0\nbuffer_load_dwordx4 v[68:71], v254, %[rs], %[h2] offen sc1\n" \
  "v_mfma_f32_16x16x32_bf16 v[180:183], v[4:7], v[132:135], 0\nv_max3_u32 v192, v192, v132, v133\nv_mfma_f32_16x16x32_bf16 v[184:187], v[8:11], v[132:135], 0\nv_max3_u32 v192, v192, v134, v135\n" \
  "v_mfma_f32_16x16x32_bf16 v[188:191], v[12:15], v[132:135], 0\ns_waitcnt vmcnt(8)\nv_mfma_f32_16x16x32_bf16 v[176:179], v[16:19], v[136:139], v[176:179]\n" \
  "buffer_load_dwordx4 v[72:75], v254, %[rs], %[h3] offen sc1\nv_mfma_f32_16x16x32_bf16 v[180:183], v[20:23], v[136:139], v[180:183]\nv_max3_u32 v192, v192, v136, v137\nv_mfma_f32_16x16x32_bf16 v[184:187], v[232:235], v[136:139], v[184:187]\n" \
  "v_max3_u32 v192, v192, v138, v139\nv_mfma_f32_16x16x32_bf16 v[188:191], v[236:239], v[136:139], v[188:191]\ns_mov_b32 %[n], 0x400000\n75:\n" \
  "ds_read_b32 v193, v194 offset:12\ns_waitcnt lgkmcnt(0)\nv_readfirstlane_b32 %[t], v193\ns_cmp_ge_u32 %[t], 8\n" \
  "s_cbranch_scc1 76f\ns_sleep 1\ns_sub_u32 %[n], %[n], 1\ns_cbranch_scc0 75b\n" \
  "76:\ns_waitcnt vmcnt(8)\nv_mfma_f32_16x16x32_bf16 v[176:179], v[240:243], v[140:143], v[176:179]\nbuffer_load_dwordx4 v[76:79], v254, %[rs], %[h4] offen sc1\n" \
  "v_mfma_f32_16x16x32_bf16 v[180:183], v[244:247], v[140:143], v[180:183]\nv_max3_u32 v192, v192, v140, v141\nv_mfma_f32_16x16x32_bf16 v[184:187], v[32:35], v[140:143], v[184:187]\nv_max3_u32 v192, v192, v142, v143\n" \
  "ds_write_b128 v196, v[160:163] offset:0\nv_mfma_f32_16x16x32_bf16 v[188:191], v[36:39], v[140:143], v[188:191]\ns_waitcnt vmcnt(8)\nv_mfma_f32_16x16x32_bf16 v[176:179], v[40:43], v[144:147], v[176:179]\n" \
  "buffer_load_dwordx4 v[80:83], v254, %[rs], %[h5] offen sc1\nv_mfma_f32_16x16x32_bf16 v[180:183], v[44:47], v[144:147], v[180:183]\nv_max3_u32 v192, v192, v144, v145\nv_mfma_f32_16x16x32_bf16 v[184:187], v[48:51], v[144:147], v[184:187]\n" \
  "v_max3_u32 v192, v192, v146, v147\nds_write_b128 v196, v[164:167] offset:1024\nv_mfma_f32_16x16x32_bf16 v[188:191], v[52:55], v[144:147], v[188:191]\ns_waitcnt vmcnt(8)\n" \
  "v_mfma_f32_16x16x32_bf16 v[176:179], v[56:59], v[148:151], v[176:179]\nbuffer_load_dwordx4 v[84:87], v254, %[rs], %[h6] offen sc1\nv_mfma_f32_16x16x32_bf16 v[180:183], v[88:91], v[148:151], v[180:183]\nv_max3_u32 v192, v192, v148, v149\n" \
  "v_mfma_f32_16x16x32_bf16 v[184:187], v[92:95], v[148:151], v[184:187]\nv_max3_u32 v192, v192, v150, v151\nds_write_b128 v196, v[168:171] offset:2048\nv_mfma_f32_16x16x32_bf16 v[188:191], v[96:99], v[148:151], v[188:191]\n" \
  "s_waitcnt vmcnt(8)\nv_mfma_f32_16x16x32_bf16 v[176:179], v[100:103], v[152:155], v[176:179]\nbuffer_load_dwordx4 v[132:135], v255, %[rs], %[h0] offen sc1\nv_mfma_f32_16x16x32_bf16 v[180:183], v[104:107], v[152:155], v[180:183]\n" \
  "v_max3_u32 v192, v192, v152, v153\nv_mfma_f32_16x16x32_bf16 v[184:187], v[108:111], v[152:155], v[184:187]\nv_max3_u32 v192, v192, v154, v155\nds_write_b128 v196, v[172:175] offset:3072\n" \
  "v_mfma_f32_16x16x32_bf16 v[188:191], v[112:115], v[152:155], v[188:191]\ns_waitcnt vmcnt(8)\nv_mfma_f32_16x16x32_bf16 v[176:179], v[116:119], v[156:159], v[176:179]\nbuffer_load_dwordx4 v[136:139], v255, %[rs], %[h1] offen sc1\n" \
  "v_mfma_f32_16x16x32_bf16 v[180:183], v[120:123], v[156:159], v[180:183]\nv_max3_u32 v192, v192, v156, v157\nv_mfma_f32_16x16x32_bf16 v[184:187], v[124:127], v[156:159], v[184:187]\nv_max3_u32 v192, v192, v158, v159\n" \
  "v_mfma_f32_16x16x32_bf16 v[188:191], v[128:131], v[156:159], v[188:191]\ns_waitcnt vmcnt(8)\nv_mfma_f32_16x16x32_bf16 v[160:163], v[0:3], v[60:63], 0\nbuffer_load_dwordx4 v[140:143], v255, %[rs], %[h2] offen sc1\n" \
  "v_mfma_f32_16x16x32_bf16 v[164:167], v[4:7], v[60:63], 0\nv_max3_u32 v192, v192, v60, v61\nv_mfma_f32_16x16x32_bf16 v[168:171], v[8:11], v[60:63], 0\nv_max3_u32 v192, v192, v62, v63\n" \
  "v_mfma_f32_16x16x32_bf16 v[172:175], v[12:15], v[60:63], 0\ns_waitcnt vmcnt(8)\nv_mfma_f32_16x16x32_bf16 v[160:163], v[16:19], v[64:67], v[160:163]\nbuffer_load_dwordx4 v[144:147], v255, %[rs], %[h3] offen sc1\n" \
  "v_mfma_f32_16x16x32_bf16 v[164:167], v[20:23], v[64:67], v[164:167]\nv_max3_u32 v192, v192, v64, v65\nv_mfma_f32_16x16x32_bf16 v[168:171], v[232:235], v[64:67], v[168:171]\nv_max3_u32 v192, v192, v66, v67\n" \
  "v_mfma_f32_16x16x32_bf16 v[172:175], v[236:239], v[64:67], v[172:175]\ns_waitcnt vmcnt(8)\nv_mfma_f32_16x16x32_bf16 v[160:163], v[240:243], v[68:71], v[160:163]\nbuffer_load_dwordx4 v[148:151], v255, %[rs], %[h4] offen sc1\n" \
  "v_mfma_f32_16x16x32_bf16 v[164:167], v[244:247], v[68:71], v[164:167]\nv_max3_u32 v192, v192, v68, v69\nv_mfma_f32_16x16x32_bf16 v[168:171], v[32:35], v[68:71], v[168:171]\nv_max3_u32 v192, v192, v70, v71\n" \
  "ds_write_b128 v196, v[176:179] offset:4096\nv_mfma_f32_16x16x32_bf16 v[172:175], v[36:39], v[68:71], v[172:175]\ns_waitcnt vmcnt(8)\nv_mfma_f32_16x16x32_bf16 v[160:163], v[40:43], v[72:75], v[160:163]\n" \
  "buffer_load_dwordx4 v[152:155], v255, %[rs], %[h5] offen sc1\nv_mfma_f32_16x16x32_bf16 v[164:167], v[44:47], v[72:75], v[164:167]\nv_max3_u32 v192, v192, v72, v73\nv_mfma_f32_16x16x32_bf16 v[168:171], v[48:51], v[72:75], v[168:171]\n" \
  "v_max3_u32 v192, v192, v74, v75\nds_write_b128 v196, v[180:183] offset:5120\nv_mfma_f32_16x16x32_bf16 v[172:175], v[52:55], v[72:75], v[172:175]\ns_waitcnt vmcnt(8)\n" \
  "v_mfma_f32_16x16x32_bf16 v[160:163], v[56:59], v[76:79], v[160:163]\nbuffer_load_dwordx4 v[156:159], v255, %[rs], %[h6] offen sc1\nv_mfma_f32_16x16x32_bf16 v[164:167], v[88:91], v[76:79], v[164:167]\nv_max3_u32 v192, v192, v76, v77\n" \
  "v_mfma_f32_16x16x32_bf16 v[168:171], v[92:95], v[76:79], v[168:171]\nv_max3_u32 v192, v192, v78, v79\nds_write_b128 v196, v[184:187] offset:6144\nv_mfma_f32_16x16x32_bf16 v[172:175], v[96:99], v[76:79], v[172:175]\n" \
  "s_waitcnt vmcnt(8)\nv_mfma_f32_16x16x32_bf16 v[160:163], v[100:103], v[80:83], v[160:163]\nv_mfma_f32_16x16x32_bf16 v[164:167], v[104:107], v[80:83], v[164:167]\nv_max3_u32 v192, v192, v80, v81\n" \
  "v_mfma_f32_16x16x32_bf16 v[168:171], v[108:111], v[80:83], v[168:171]\nv_max3_u32 v192, v192, v82, v83\nds_write_b128 v196, v[188:191] offset:7168\nv_mfma_f32_16x16x32_bf16 v[172:175], v[112:115], v[80:83], v[172:175]\n" \
  "s_waitcnt vmcnt(7)\nv_mfma_f32_16x16x32_bf16 v[160:163], v[116:119], v[84:87], v[160:163]\nv_mfma_f32_16x16x32_bf16 v[164:167], v[120:123], v[84:87], v[164:167]\nv_max3_u32 v192, v192, v84, v85\n" \
  "v_mfma_f32_16x16x32_bf16 v[168:171], v[124:127], v[84:87], v[168:171]\nv_max3_u32 v192, v192, v86, v87\nv_mfma_f32_16x16x32_bf16 v[172:175], v[128:131], v[84:87], v[172:175]\ns_waitcnt vmcnt(6)\n" \
  "v_mfma_f32_16x16x32_bf16 v[176:179], v[0:3], v[132:135], 0\nv_mfma_f32_16x16x32_bf16 v[180:183], v[4:7], v[132:135], 0\nv_max3_u32 v192, v192, v132, v133\nv_mfma_f32_16x16x32_bf16 v[184:187], v[8:11], v[132:135], 0\n" \
  "v_max3_u32 v192, v192, v134, v135\nv_mfma_f32_16x16x32_bf16 v[188:191], v[12:15], v[132:135], 0\ns_waitcnt vmcnt(5)\nv_mfma_f32_16x16x32_bf16 v[176:179], v[16:19], v[136:139], v[176:179]\n" \
  "v_mfma_f32_16x16x32_bf16 v[180:183], v[20:23], v[136:139], v[180:183]\nv_max3_u32 v192, v192, v136, v137\nv_mfma_f32_16x16x32_bf16 v[184:187], v[232:235], v[136:139], v[184:187]\nv_max3_u32 v192, v192, v138, v139\n" \
  "v_mfma_f32_16x16x32_bf16 v[188:191], v[236:239], v[136:139], v[188:191]\ns_waitcnt vmcnt(4)\nv_mfma_f32_16x16x32_bf16 v[176:179], v[240:243], v[140:143], v[176:179]\nv_mfma_f32_16x16x32_bf16 v[180:183], v[244:247], v[140:143], v[180:183]\n" \
  "v_max3_u32 v192, v192, v140, v141\nv_mfma_f32_16x16x32_bf16 v[184:187], v[32:35], v[140:143], v[184:187]\nv_max3_u32 v192, v192, v142, v143\nds_write_b128 v196, v[160:163] offset:8192\n" \
  "v_mfma_f32_16x16x32_bf16 v[188:191], v[36:39], v[140:143], v[188:191]\ns_waitcnt vmcnt(3)\nv_mfma_f32_16x16x32_bf16 v[176:179], v[40:43], v[144:147], v[176:179]\nv_mfma_f32_16x16x32_bf16 v[180:183], v[44:47], v[144:147], v[180:183]\n" \
  "v_max3_u32 v192, v192, v144, v145\nv_mfma_f32_16x16x32_bf16 v[184:187], v[48:51], v[144:147], v[184:187]\nv_max3_u32 v192, v192, v146, v147\nds_write_b128 v196, v[164:167] offset:9216\n" \
  "v_mfma_f32_16x16x32_bf16 v[188:191], v[52:55], v[144:147], v[188:191]\ns_waitcnt vmcnt(2)\nv_mfma_f32_16x16x32_bf16 v[176:179], v[56:59], v[148:151], v[176:179]\nv_mfma_f32_16x16x32_bf16 v[180:183], v[88:91], v[148:151], v[180:183]\n" \
  "v_max3_u32 v192, v192, v148, v149\nv_mfma_f32_16x16x32_bf16 v[184:187], v[92:95], v[148:151], v[184:187]\nv_max3_u32 v192, v192, v150, v151\nds_write_b128 v196, v[168:171] offset:10240\n" \
  "v_mfma_f32_16x16x32_bf16 v[188:191], v[96:99], v[148:151], v[188:191]\ns_waitcnt vmcnt(1)\nv_mfma_f32_16x16x32_bf16 v[176:179], v[100:103], v[152:155], v[176:179]\nv_mfma_f32_16x16x32_bf16 v[180:183], v[104:107], v[152:155], v[180:183]\n" \
  "v_max3_u32 v192, v192, v152, v153\nv_mfma_f32_16x16x32_bf16 v[184:187], v[108:111], v[152:155], v[184:187]\nv_max3_u32 v192, v192, v154, v155\nds_write_b128 v196, v[172:175] offset:11264\n" \
  "v_mfma_f32_16x16x32_bf16 v[188:191], v[112:115], v[152:155], v[188:191]\ns_waitcnt vmcnt(0)\nv_mfma_f32_16x16x32_bf16 v[176:179], v[116:119], v[156:159], v[176:179]\nv_mfma_f32_16x16x32_bf16 v[180:183], v[120:123], v[156:159], v[180:183]\n" \
  "v_max3_u32 v192, v192, v156, v157\nv_mfma_f32_16x16x32_bf16 v[184:187], v[124:127], v[156:159], v[184:187]\nv_max3_u32 v192, v192, v158, v159\nv_mfma_f32_16x16x32_bf16 v[188:191], v[128:131], v[156:159], v[188:191]\n" \
  "v_cmp_eq_u32 vcc, -1, v192\ns_nop 5\nds_write_b128 v196, v[176:179] offset:12288\nds_write_b128 v196, v[180:183] offset:13312\n" \
  "ds_write_b128 v196, v[184:187] offset:14336\nds_write_b128 v196, v[188:191] offset:15360\ns_cbranch_vccz 81f\nbuffer_load_dwordx4 v[60:63], v252, %[rs], %[h0] offen sc1\n" \
  "buffer_load_dwordx4 v[64:67], v252, %[rs], %[h1] offen sc1\nbuffer_load_dwordx4 v[68:71], v252, %[rs], %[h2] offen sc1\nbuffer_load_dwordx4 v[72:75], v252, %[rs], %[h3] offen sc1\nbuffer_load_dwordx4 v[76:79], v252, %[rs], %[h4] offen sc1\n" \
  "buffer_load_dwordx4 v[80:83], v252, %[rs], %[h5] offen sc1\nbuffer_load_dwordx4 v[84:87], v252, %[rs], %[h6] offen sc1\ns_waitcnt vmcnt(0)\ns_sub_u32 %[rp], %[rp], 1\n" \
  "s_cbranch_scc0 80b\n81:\n"

__global__ void __launch_bounds__(512, 2)
fused_block(const u16* x, const u16* wq, const u16* wk, const u16* wv,
            const u16* wo, const u16* w1, const u16* w2, const u16* cvec,
            const u16* wmod, unsigned char* ws, u16* out) {
  __shared__ __attribute__((aligned(16))) unsigned char lds[L_END];
  LAS unsigned char* L = (LAS unsigned char*)lds;
  const unsigned lds0 = (unsigned)(uintptr_t)lds;
  const int bid = blockIdx.x, tid = threadIdx.x, lane = tid & 63, w = __builtin_amdgcn_readfirstlane(tid >> 6);
  const int rb = (bid & 7) * 4 + ((bid >> 3) & 3), m = bid >> 5;
  const int g = lane >> 4, c = lane & 15, hi = lane >> 5, c32 = lane & 31;
#define XROW(i) (16 * ((((i) >> 1) + m) & 3) + 2 * w + ((i) & 1))
  unsigned l16 = lane * 16;
  const unsigned mvo = (tid >> 3) * 6144 + (tid & 7) * 16, cvo = (tid >> 3) * 2;
  unsigned mv0 = mvo + (bid >> 4) * 128 * 6144 + (bid & 15) * 128, mv1 = mv0 + 64 * 6144, cv0 = cvo + (bid >> 4) * 256;
  unsigned fo = tid * 16 + (w == 0 ? WS_KV + bid * 1024 : WS_MODP + bid * 768 - 1024) + (tid < 112 ? 0 : WS_END);
  u32x4 sn = {SENT, SENT, SENT, SENT};
  int xo[4];
#pragma unroll
  for (int i = 0; i < 4; ++i) { xo[i] = (64 * rb + XROW(2 * i)) * 1024; PINS(xo[i]); }
  asm volatile("" : "+v"(l16), "+v"(mv0), "+v"(mv1), "+v"(cv0), "+v"(fo), "+v"(sn));
  SBAR();
  asm volatile("" : "+s"(x), "+s"(wq), "+s"(wk), "+s"(wv), "+s"(wo), "+s"(w1), "+s"(w2), "+s"(cvec), "+s"(wmod), "+s"(ws), "+s"(out));
  u32x2 epv;
  asm volatile("global_load_dwordx2 %0, %1, %2 sc1" : "=&v"(epv) : "v"(0u), "s"(ws) : "memory");
#define EPOCH() ({ unsigned lo_, hi_; asm volatile("v_readfirstlane_b32 %0, %2\n\tv_readfirstlane_b32 %1, %3\n\ts_nop 1" : "=s"(lo_), "=s"(hi_) : "v"(epv[0]), "v"(epv[1])); \
                   (((u64)hi_ << 32) | lo_) + EPOCH_STEP; })
  const __amdgpu_buffer_rsrc_t rs = __builtin_amdgcn_make_buffer_rsrc(ws, 0, WS_END, 0x00020000);

  const bool crit = bid < 64;
  const bool late = (bid >> 6) == 1, kvwg = bid >= 128;
  const unsigned l16b = l16 + 8192;
#define BL16(D, R, V, S) asm volatile("buffer_load_dwordx4 %0, %1, %2, %3 offen" : "=&v"(D) : "v"(V), "s"(R), "s"(S) : "memory")
  u32x4 wm[4];
  unsigned cv[4];
  const __amdgpu_buffer_rsrc_t xs = __builtin_amdgcn_make_buffer_rsrc((void*)x, 0, 2048 * 512 * 2, 0x00020000);
  u32x4 xr[8];
#define X_LD(i) asm volatile("buffer_load_dwordx4 %0, %2, %3, %4 offen\n\tbuffer_load_dwordx4 %1, %2, %3, %4 offen offset:1024" \
                             : "=&v"(xr[2 * (i)]), "=&v"(xr[2 * (i) + 1]) : "v"(l16), "s"(xs), "s"(xo[i]) : "memory")
#define SILU(v) ((v) * __builtin_amdgcn_rcpf(1.f + __builtin_amdgcn_exp2f((v) * -1.4426950408889634f)))
#define MOD_PART(N) do { \
    float p[8] = {0.f, 0.f, 0.f, 0.f, 0.f, 0.f, 0.f, 0.f}; \
    _Pragma("unroll") for (int i = 0; i < N; ++i) { const unsigned sl = pack2(SILU(bflo(cv[i])), 0.f), sh_ = sl << 16; \
      _Pragma("unroll") for (int q = 0; q < 4; ++q) { p[2 * q] = dot2bf(wm[i][q], sl, p[2 * q]); p[2 * q + 1] = dot2bf(wm[i][q], sh_, p[2 * q + 1]); } } \
    float t0 = fold16(fold32(p[0], p[4]), fold32(p[2], p[6])), t1 = fold16(fold32(p[1], p[5]), fold32(p[3], p[7])); \
    t0 += dpp<0x128>(t0); t1 += dpp<0x128>(t1); \
    if (c < 8) *(LAS f32x2*)(L + L_RED + (w * 64 + c * 8 + 2 * g) * 4) = (f32x2){t0, t1}; } while (0)
#define MOD_SUM() ({ float s_ = 0.f; _Pragma("unroll") for (int i = 0; i < 8; ++i) s_ += *(const LAS float*)(L + L_RED + (i * 64 + lane) * 4); s_; })
  {
    asm volatile("s_cmp_ge_u32 %[b], 64\ns_cbranch_scc1 1f\n"
                 "global_load_dwordx4 %0, %[v0], %[pw]\nglobal_load_dwordx4 %1, %[v1], %[pw]\n"
                 "global_load_ushort %2, %[vc], %[pc]\nglobal_load_ushort %3, %[vc], %[pc] offset:128\n"
                 "s_cmp_ge_u32 %[w], 2\ns_cbranch_scc1 1f\nbuffer_store_dwordx4 %[sn], %[fo], %[rs], 0 offen sc1\ns_nop 1\n1:\n"
                 : "=&v"(wm[0]), "=&v"(wm[1]), "=&v"(cv[0]), "=&v"(cv[1])
                 : [b] "s"(bid), [w] "s"(w), [rs] "s"(rs), [sn] "v"(sn), [fo] "v"(fo),
                   [v0] "v"(mv0), [v1] "v"(mv1), [vc] "v"(cv0), [pw] "s"(wmod), [pc] "s"(cvec) : "memory", "scc");
    X_LD(0); X_LD(1);
  }
  asm volatile("s_cmp_ge_u32 %4, 64\ns_cbranch_scc1 1f\ns_waitcnt vmcnt(4)\n1:\n" : "+v"(wm[0]), "+v"(wm[1]), "+v"(cv[0]), "+v"(cv[1]) : "s"(bid) : "memory", "scc");
  X_LD(2); X_LD(3);
  if (crit) {
    MOD_PART(2);
    __syncthreads();
    const float s = MOD_SUM();
    const u64 gr = (EPOCH() & ~0xffffffull) | ((__float_as_uint(s) + 0x80u) >> 8);
    GAS u64* gp = (GAS u64*)(ws + WS_MODG + w * 32768 + (bid >> 4) * 8192 + ((bid & 15) * 64 + lane) * 8);
    __hip_atomic_store(gp, gr, RLX_AGENT);
    __hip_atomic_store(gp + 32768, gr, RLX_AGENT);
  }
  {
    u64 pc; unsigned clen;
    asm volatile("s_getpc_b64 %0\n.Lfb_code_lo:\ns_mov_b32 %1, .Lfb_code_hi-.Lfb_code_lo" : "=s"(pc), "=s"(clen));
    const __amdgpu_buffer_rsrc_t a0 = __builtin_amdgcn_make_buffer_rsrc(late ? (void*)wmod : (kvwg ? (void*)wk : (void*)pc), 0, late ? 512 * 3072 * 2 : (kvwg ? 16384 : clen), 0x00020000);
    const __amdgpu_buffer_rsrc_t a1 = __builtin_amdgcn_make_buffer_rsrc(late ? (void*)wmod : (void*)wv, 0, late ? 512 * 3072 * 2 : (kvwg ? 16384 : 0), 0x00020000);
    const unsigned vo = late ? mvo : w * 2048 + l16 + (kvwg ? 0 : (bid >> 3) * 16384);
    const int s0 = late ? (m & 1) * 256 * 6144 + 2048 + (bid & 31) * 128 : 0, sd = late ? 64 * 6144 : 1024, s2 = late ? s0 + 128 * 6144 : 0;
    BL16(wm[0], a0, vo, s0); BL16(wm[1], a0, vo, s0 + sd); BL16(wm[2], a1, vo, s2); BL16(wm[3], a1, vo, s2 + sd);
    asm volatile("s_cmp_lg_u32 %[b], 1\ns_cbranch_scc1 1f\nglobal_load_ushort %0, %[vc], %[pc]\nglobal_load_ushort %1, %[vc], %[pc] offset:128\n"
                 "global_load_ushort %2, %[vc], %[pc] offset:256\nglobal_load_ushort %3, %[vc], %[pc] offset:384\n1:\n"
                 : "=&v"(cv[0]), "=&v"(cv[1]), "=&v"(cv[2]), "=&v"(cv[3]) : [b] "s"(bid >> 6), [vc] "v"(cvo), [pc] "s"(cvec + (m & 1) * 256) : "memory", "scc");
  }
  const u16* gsrc; int gld, gdst; unsigned gvo, gt0, gt1;
  {
    const int pi = bid * 8 + w;
    int row0, col;
    gt0 = 8 * g; gt1 = 256;
    if (pi < 256) { const int mm = pi >> 5, ks = (pi >> 1) & 15;
      gsrc = wo; gld = 1024; gdst = WS_WO + (pi ^ (ks >> 3)) * 2048;
      row0 = 32 * ks; col = 64 * mm + 32 * (pi & 1); gt0 = 16 * (g >> 1) + 4 * (g & 1); gt1 = 512; }
    else if (pi < 1152) { const int id = pi - 256;
      gsrc = w1; gld = 2 * F_; gdst = WS_W1 + id * 2048;
      row0 = 32 * (id & 15); col = 32 * (id >> 4); }
    else { const int id = pi - 1152, mm = id / 112, n2 = (id - mm * 112) >> 1;
      const int kk = n2 % 7 - mm % 7;
      gsrc = w2; gld = 1024; gdst = WS_W2 + ((mm * 56 + n2 - n2 % 7 + (kk < 0 ? kk + 7 : kk)) * 2 + (id & 1)) * 2048;
      row0 = 32 * n2; col = 64 * mm + 32 * (id & 1); }
    gvo = (row0 + (lane >> 2)) * gld + col * 2 + (lane & 3) * 16;
    gt0 = lds0 + L_WKV + w * 2048 + (gt0 + (c >> 2)) * 64 + (c & 3) * 16; gt1 += gt0;
  }
  PINS(gld); PINS(gdst); PINS(gsrc); PIN(gvo); PIN(gt0); PIN(gt1);
  {
    const unsigned ov = (c & 11) == 0 ? 0x3f803f80u : 0u;
    *(LAS u32x4*)(L + L_ONES + tid * 16) = (u32x4){ov, ov, ov, ov};
    if (tid < 8) *(LAS unsigned*)(L + L_CNT + tid * 4) = 0u;
  }
  const __amdgpu_buffer_rsrc_t qs = __builtin_amdgcn_make_buffer_rsrc((void*)wq, 0, 512 * 512 * 2, 0x00020000);
  unsigned qvo = (tid >> 3) * 1024 + m * 128 + (((tid & 7) ^ (((tid >> 3) & 2) | ((tid >> 4) & 4))) << 4), qso = ((2 * (bid >> 3)) & 7) * 65536, qld = lds0 + w * 1024;
  PIN(qvo); PINS(qso); PINS(qld);
#define WQ_DMA1() asm volatile("s_lshr_b32 m0, %0, 3\n\ts_add_u32 m0, m0, %3\n\ts_nop 0\n\tbuffer_load_dwordx4 %1, %2, %0 offen lds\n\t" \
                               "s_add_u32 %0, %0, 0x10000\n\ts_and_b32 %0, %0, 0x7ffff" : "+s"(qso) : "v"(qvo), "s"(qs), "s"(qld) : "memory", "scc")
  SBAR();
  asm volatile("s_waitcnt vmcnt(6)\ns_cmp_lt_u32 %8, 128\ns_cbranch_scc1 1f\ns_waitcnt vmcnt(4)\n1:\n"
               : "+v"(xr[0]), "+v"(xr[1]), "+v"(xr[2]), "+v"(xr[3]), "+v"(xr[4]), "+v"(xr[5]), "+v"(xr[6]), "+v"(xr[7]) : "s"(bid) : "memory", "scc");
  const u64 epoch = EPOCH();
  float rsd[8], nb[8];
  ln_stats(xr, rsd, nb);
  const int hh = w >> 1, rt = w & 1;
  asm volatile("" : "+s"(rsd[0]), "+s"(rsd[1]), "+s"(nb[6]), "+s"(nb[7]) :: "memory");
  SBAR();
  asm volatile("s_cmp_lt_u32 %8, 128\ns_cbranch_scc1 1f\ns_waitcnt vmcnt(0)\n1:\n"
               : "+v"(wm[0]), "+v"(wm[1]), "+v"(wm[2]), "+v"(wm[3]), "+v"(cv[0]), "+v"(cv[1]), "+v"(cv[2]), "+v"(cv[3]) : "s"(bid) : "memory", "scc");
  u32x4 kfr[4];
  {
    if (kvwg) {
#pragma unroll
      for (int i = 0; i < 4; ++i) *(LAS u32x4*)(L + L_H1 + w * 4096 + i * 1024 + lane * 16) = wm[i];
      const LAS unsigned char* b0 = L + L_H1 + w * 4096 + (8 * g + (c >> 2)) * 32 + (c & 3) * 8;
      asm volatile("s_waitcnt lgkmcnt(0)" ::: "memory");
#pragma unroll
      for (int i = 0; i < 4; ++i) {
        const u32x2 lo = tr_read(b0 + i * 1024), hi2 = tr_read(b0 + i * 1024 + 128);
        kfr[i] = (u32x4){lo[0], lo[1], hi2[0], hi2[1]};
      }
    }
  }
  u32x4 mg[4];
#define RELOAD(D, V, S) asm volatile("buffer_load_dwordx4 %0, %1, %2, %3 offen sc1\n\ts_waitcnt vmcnt(0)" : "=&v"(D) : "v"(V), "s"(rs), "s"(S) : "memory")
#define BL16C(D, V, S) asm volatile("buffer_load_dwordx4 %0, %1, %2, %3 offen sc1" : "=&v"(D) : "v"(V), "s"(rs), "s"(S) : "memory")
  f32x2 xh[8][4];
#define XHAT(i, q) (__builtin_elementwise_fma((f32x2){bflo(xr[i][q]), bfhi(xr[i][q])}, (f32x2){rsd[i], rsd[i]}, (f32x2){nb[i], nb[i]}))
#pragma unroll
  for (int i = 0; i < 8; ++i) {
    if (i < 4) { WQ_DMA1(); WQ_DMA1(); }
#pragma unroll
    for (int q = 0; q < 4; ++q) xh[i][q] = XHAT(i, q);
    asm volatile("" : "+v"(xh[i][0]), "+v"(xh[i][1]), "+v"(xh[i][2]), "+v"(xh[i][3]));
    SBAR();
  }
  u32x4 wf[16];
  const LAS unsigned char* a0 = L + (8 * g + (c >> 2)) * 128 + ((hh ^ (((c >> 3) & 1) | ((g & 1) << 1))) << 5) + (c & 3) * 8;
#define W_RD(K0, K1) _Pragma("unroll") for (int ks = K0; ks < K1; ++ks) { \
      const u32x2 lo = tr_read(a0 + ks * 4096), hi2 = tr_read(a0 + ks * 4096 + 512); wf[ks] = (u32x4){lo[0], lo[1], hi2[0], hi2[1]}; }
#define MG_LD() _Pragma("unroll") for (int k = 0; k < 4; ++k) BL16C(mg[k], tid * 16, WS_MODG + (bid & 15) * 32768 + k * 8192)
  DRAIN_BAR();
  MG_LD();
  W_RD(0, 16)
  SBAR();
  asm volatile("s_waitcnt vmcnt(0)" : "+v"(mg[0]), "+v"(mg[1]), "+v"(mg[2]), "+v"(mg[3]) :: "memory");
  asm volatile("" : "+v"(wm[0]), "+v"(wm[1]), "+v"(wm[2]), "+v"(wm[3]), "+v"(cv[0]), "+v"(cv[1]), "+v"(cv[2]), "+v"(cv[3]));
  {
    const unsigned thi = (unsigned)(epoch >> 32), tlo = (unsigned)epoch;
#pragma unroll 1
    for (int spins = 0; spins < SPIN_CAP; ++spins) {
      unsigned bad = 0u;
#pragma unroll
      for (int i = 0; i < 4; ++i) bad |= (mg[i][1] ^ thi) | (mg[i][3] ^ thi) | (((mg[i][0] ^ tlo) | (mg[i][2] ^ tlo)) & 0xff000000u);
      if (__all(bad == 0u)) break;
      __builtin_amdgcn_s_sleep(2);
      CFENCE();
#pragma unroll
      for (int i = 0; i < 4; ++i) mg[i] = LD16(tid * 16, WS_MODG + (bid & 15) * 32768 + i * 8192);
    }
    const float a0 = (__uint_as_float(mg[0][0] << 8) + __uint_as_float(mg[1][0] << 8)) + (__uint_as_float(mg[2][0] << 8) + __uint_as_float(mg[3][0] << 8));
    const float a1 = (__uint_as_float(mg[0][2] << 8) + __uint_as_float(mg[1][2] << 8)) + (__uint_as_float(mg[2][2] << 8) + __uint_as_float(mg[3][2] << 8));
    unsigned t = pack2(a0, a1);
    if (w >= 4) t = pack2(1.f + bflo(t), 1.f + bfhi(t));
    *(LAS unsigned*)(L + L_MODA + tid * 4) = t;
  }
  __syncthreads();
  typedef unsigned u32x8 __attribute__((ext_vector_type(8)));
  u32x8 ge;
  unsigned kn = 0u;
  {
    const u32x4 sa = *(const LAS u32x4*)(L + L_MODA + lane * 16), ca = *(const LAS u32x4*)(L + L_MODA + 1024 + lane * 16);
    f32x2 sc[4], sh[4];
#pragma unroll
    for (int q = 0; q < 4; ++q) { sc[q] = (f32x2){bflo(ca[q]), bfhi(ca[q])}; sh[q] = (f32x2){bflo(sa[q]), bfhi(sa[q])}; }
#define H_ROW(i) do { u32x4 o_; \
      _Pragma("unroll") for (int q = 0; q < 4; ++q) { const f32x2 t_ = __builtin_elementwise_fma(xh[i][q], sc[q], sh[q]); o_[q] = pack2(t_[0], t_[1]); } \
      *(LAS u32x4*)(L + L_H1 + XROW(i) * HROW + lane * 16) = o_; } while (0)
    H_ROW(0); H_ROW(1);
    if (kvwg) {
      __syncthreads();
      const int sub = m - 4;
      const LAS unsigned char* hp = L + L_H1 + (16 * sub + c) * HROW + g * 16 + w * 128;
      const u32x4 h0 = *(const LAS u32x4*)hp, h1 = *(const LAS u32x4*)(hp + 64);
      f32x4 ak = {0.f, 0.f, 0.f, 0.f}, av = ak;
      ak = mfma16(kfr[0], h0, ak); av = mfma16(h0, kfr[2], av);
      ak = mfma16(kfr[1], h1, ak); av = mfma16(h1, kfr[3], av);
      *(LAS f32x4*)(L + L_WKV + w * 2048 + lane * 16) = ak;
      *(LAS f32x4*)(L + L_WKV + w * 2048 + 1024 + lane * 16) = av;
      __syncthreads();
      if (w < 2) {
        ak = *(const LAS f32x4*)(L + L_WKV + w * 1024 + lane * 16);
#pragma unroll
        for (int i = 1; i < 8; ++i) ak += *(const LAS f32x4*)(L + L_WKV + i * 2048 + w * 1024 + lane * 16);
        const u32x2 ok = {min(pack2(ak[0], ak[1]), SENT - 1u), min(pack2(ak[2], ak[3]), SENT - 1u)};
        const int tile = 2 * rb + (sub >> 1), s = sub & 1;
        const int kvo = WS_KV + (tile >> 3) * 16384 + (tile & 7) * 1024 + (w ? 8192 + s * 512 : s * 256);
        __builtin_amdgcn_raw_buffer_store_b64(ok, rs, w ? (g & 1) * 256 + c * 16 + (g >> 1) * 8 : ((g >> 1) * 32 + c) * 16 + (g & 1) * 8, kvo, AUX_SC1);
        if (w == 0) {
          float n2 = dot2bf(ok[0], ok[0], dot2bf(ok[1], ok[1], 0.f));
          n2 = fold32(n2, n2);
          n2 = fold16(n2, n2);
          kn = min(wave_umax(__float_as_uint(n2)), SENT - 1u);
        }
      }
    }
    asm volatile("global_load_dwordx4 v[32:35], %1, %3\n\tglobal_load_dwordx4 v[36:39], %2, %3"
                 : "=&{v[32:39]}"(ge) : "v"(gvo), "v"(gvo + 16 * gld), "s"(gsrc) : "memory");
    H_ROW(2); H_ROW(3); H_ROW(4); H_ROW(5); H_ROW(6); H_ROW(7);
    if (late) MOD_PART(4);
    __syncthreads();
    if (late && w == 0)
      __builtin_amdgcn_raw_buffer_store_b32(min(__float_as_uint(MOD_SUM()), SENT - 1u), rs, lane * 4, WS_MODP + (m & 1) * 8192 + (bid & 31) * 256, AUX_SC1);
  }
  const int kvw = (w >> 1) * 16384 + (w & 1) * 4096;
  u32x4 qf;
  {
    f32x4 acc0 = {0.f, 0.f, 0.f, 0.f}, acc1 = acc0;
    const LAS unsigned char* hp = L + L_H1 + (32 * rt + c) * HROW + g * 16;
    u32x4 b0[16], b1[16];
#define Q_RD(ks) do { b0[ks] = *(const LAS u32x4*)(hp + (ks) * 64); b1[ks] = *(const LAS u32x4*)(hp + 16 * HROW + (ks) * 64); } while (0)
#define KV_DMA(i) asm volatile("s_mov_b32 m0, %2\n\ts_nop 0\n\tglobal_load_lds_dwordx4 %0, %1 offset:%3 sc1" \
                               :: "v"((i) < 4 ? l16 : l16b), "s"(ws + WS_KV + kvw), "s"(lds0 + kvw + ((i) >> 2) * 8192), "i"(((i) & 3) * 1024) : "memory")
    Q_RD(0); Q_RD(1); Q_RD(2); Q_RD(3);
    SBAR();
#pragma unroll
    for (int ks = 0; ks < 16; ++ks) {
      if (ks == KV0 + 6)
        asm volatile("s_cmp_lt_i32 %[go], 0\ns_cbranch_scc1 1f\ns_waitcnt vmcnt(8)\ns_mov_b64 exec, 0xffff\n"
                     "buffer_store_dword %[kn], %[vo], %[rs], %[so] offen sc1\ns_mov_b64 exec, -1\n1:\n"
                     :: [go] "s"((bid - 128) | (1 - w)), [kn] "v"(kn), [vo] "v"(l16 * 64), [rs] "s"(rs), [so] "s"(WS_KF + ((bid - 128) * 2 + w) * 4) : "memory", "scc");
      if (ks >= KV0 && ks < KV0 + 8) KV_DMA((ks - KV0) & 7);
      if (ks + 4 < 16) Q_RD(ks + 4);
      acc0 = mfma16(wf[ks], b0[ks], acc0);
      acc1 = mfma16(wf[ks], b1[ks], acc1);
      SBAR();
    }
    auto r0 = __builtin_amdgcn_permlane16_swap(pack2(acc0[0] * QSCALE, acc0[1] * QSCALE), pack2(acc1[0] * QSCALE, acc1[1] * QSCALE), false, false);
    auto r1 = __builtin_amdgcn_permlane16_swap(pack2(acc0[2] * QSCALE, acc0[3] * QSCALE), pack2(acc1[2] * QSCALE, acc1[3] * QSCALE), false, false);
    qf = (u32x4){r0[0], r1[0], r0[1], r1[1]};
  }
  float q2 = knorm2(qf);
  q2 = fold32(q2, q2);
  f32x16 o, cin;
#pragma unroll
  for (int r = 0; r < 16; ++r) o[r] = 0.f;
  unsigned kp = lds0 + lane * 16;
  unsigned vp = lds0 + ((c32 < 16) ? (8192 + hi * 256 + c32 * 16) : (L_ONES + (c32 - 16) * 16));
  unsigned vstep = (c32 < 16) ? 16384 : 0;
  u32x4 v1 = {0u, 0u, 0u, 0u}, p1 = {0u, 0u, 0u, 0u};
  asm volatile("" : "+{v[176:191]}"(o), "+{v[236:239]}"(v1), "+{v[244:247]}"(p1), "+{v248}"(kp), "+{v249}"(vp), "+{v250}"(vstep));
  PIN(q2);
  SBAR();
  {
    u32x4 kvs[8];
    DRAIN();
#pragma unroll 1
    for (int spins = 0; spins < SPIN_CAP; ++spins) {
      unsigned mx = 0u;
#pragma unroll
      for (int i = 0; i < 8; ++i) kvs[i] = *(const LAS u32x4*)(L + kvw + (i >> 2) * 8192 + (i & 3) * 1024 + lane * 16);
#pragma unroll
      for (int i = 0; i < 8; ++i) mx = max(mx, umax4(kvs[i]));
      if (__builtin_expect(!__any(mx == SENT), 1)) break;
      __builtin_amdgcn_s_sleep(1);
#pragma unroll
      for (int i = 0; i < 8; ++i) KV_DMA(i);
      DRAIN();
    }
    unsigned km = max(__float_as_uint(fold32(knorm2(kvs[0]), knorm2(kvs[1]))), __float_as_uint(fold32(knorm2(kvs[2]), knorm2(kvs[3]))));
    km = max(km, dppu<0xB1>(km)); km = max(km, dppu<0x4E>(km)); km = max(km, dppu<0x141>(km)); km = max(km, dppu<0x140>(km));
    if (c == 0) asm volatile("ds_max_u32 %0, %1" :: "v"(lds0 + L_CNT + 28), "v"(km) : "memory");
  }
  u32x4 kfl;
  asm volatile("s_waitcnt lgkmcnt(0)\n\ts_barrier" ::: "memory");
  const float boundA = __builtin_amdgcn_sqrtf(q2 * *(const LAS float*)(L + L_CNT + 28)) * 1.0001f;
#pragma unroll
  for (int r = 0; r < 16; ++r) cin[r] = -boundA;
  asm volatile("" : "+{v[160:175]}"(cin));
  f32x16 sA;
  u32x4 kf, v0;
  asm volatile("ds_read_b128 v[228:231], v248\nds_read_b128 v[232:235], v249\ns_waitcnt lgkmcnt(1)\n"
               "v_mfma_f32_32x32x16_bf16 v[192:207], v[228:231], v[224:227], v[160:175]\nds_read_b128 v[228:231], v248 offset:1024\n"
               "s_nop 7\ns_nop 2\ns_waitcnt lgkmcnt(0)\n"
               : "=&{v[192:207]}"(sA), "=&{v[228:231]}"(kf), "=&{v[232:235]}"(v0) : "{v[160:175]}"(cin), "{v[224:227]}"(qf), "{v248}"(kp), "{v249}"(vp) : "memory");
  __builtin_amdgcn_s_setprio(3);
  const int nt = 7 * m + (w < 7 ? w : 0);
  {
#define TRR(R, A, OFF) "ds_read_b64_tr_b16 " R ", " A " offset:" OFF "\n"
#define GS(R, B, OFF) "buffer_store_dwordx4 " R ", %[va], %[rs], " B " offen offset:" OFF " sc1\n"
#define GF(B, OFF) GS("v[48:51]", B, OFF)
#define SENT4 "v_mov_b32 v48, -1\nv_mov_b32 v49, -1\nv_mov_b32 v50, -1\nv_mov_b32 v51, -1\n"
    SBAR();
    asm volatile(SENT4
                 ATT_ASM("ds_write_b128 %[gw], v[32:35]\n", "ds_write_b128 %[gw], v[36:39] offset:1024\n",
                         TRR("v[32:33]", "%[t0]", "0") TRR("v[34:35]", "%[t1]", "0"), TRR("v[36:37]", "%[t0]", "8") TRR("v[38:39]", "%[t1]", "8"),
                         GS("v[32:35]", "%[d0]", "0"), "",
                         "", "", "buffer_load_dwordx4 v[52:55], %[va], %[rs], %[kf] offen sc1\n", "",
                         GS("v[36:39]", "%[d0]", "1024"), "", "", "",
                         GF("%[fo]", "0"), "")
                 "s_waitcnt vmcnt(2)\n"
                 : ATT_STATE, "+{v[32:39]}"(ge), "=&{v[52:55]}"(kfl)
                 : ATT_IN, [va] "v"(l16), [rs] "s"(rs), [kf] "s"(WS_KF + (bid & 15) * 1024), [d0] "s"(gdst),
                   [fo] "s"(WS_OB + ((rb * 2 + rt) * 32 + 4 * m + hh) * 1024), [gw] "v"(lds0 + L_WKV + w * 2048 + l16), [t0] "v"(gt0), [t1] "v"(gt1)
                 : ATT_CLOB, "v48", "v49", "v50", "v51");
    SBAR();
  }
#pragma unroll 1
  for (int spins = 0; spins < SPIN_CAP && __any(umax4(kfl) == SENT); ++spins) {
    __builtin_amdgcn_s_sleep(4);
    RELOAD(kfl, l16, WS_KF + (bid & 15) * 1024);
  }
  const unsigned kmB = umax4(kfl);
  __builtin_amdgcn_s_setprio(2);
  {
    SBAR();
    asm volatile("s_mov_b32 m0, %[ld]\n"
                 ATT_ASM(DMA("%[va]", "0"), DMA("%[va]", "1024"), DMA("%[va]", "2048"), DMA("%[va]", "3072") "s_add_u32 m0, m0, 0x2000\n",
                         DMA("%[vb]", "0"), DMA("%[vb]", "1024"), DMA("%[vb]", "2048"), DMA("%[vb]", "3072"), "", "", "", "", "", "", "", "") "s_waitcnt vmcnt(0)\n"
                 : ATT_STATE
                 : ATT_IN, [va] "v"(l16), [vb] "v"(l16b), [sb] "s"(ws + WS_KV + 65536 + kvw), [ld] "s"(lds0 + 65536 + kvw) : ATT_CLOB, "scc");
    SBAR();
  }
  lds_arrive_flag(L + L_CNT, ws + WS_AUXF + bid * 4, l16 * 64, 0u, 0xffffu);
  __builtin_amdgcn_s_setprio(1);
#define ATT_FILL(F0, F1, F2) do { SBAR(); \
    asm volatile(SENT4 ATT_ASM(GF("%[f0]", "0"), "", "", "", "", "", GF("%[f1]", "0"), "", "", "", "", "", GF("%[f2]", "0"), "", "", "") \
                 : ATT_STATE : ATT_IN, [va] "v"(l16), [rs] "s"(rs), [f0] "s"(F0), [f1] "s"(F1), [f2] "s"(F2) : ATT_CLOB, "v48", "v49", "v50", "v51"); SBAR(); } while (0)
  const int hbo = w < 7 ? WS_HB + (rb * 224 + nt) * 1024 : WS_END;
  ATT_FILL(WS_YB + rb * 65536 + m * 8192 + w * 1024, hbo, hbo + 56 * 1024);
  lds_wait<8>(L + L_CNT);
  {
    const float boundB = __builtin_amdgcn_sqrtf(q2 * __uint_as_float(wave_umax(kmB))) * 1.0001f;
    if (__builtin_expect(!__all(boundB - boundA <= 16.f), 0)) {
      const float nb2 = fmaxf(boundB, boundA), dl = boundA - nb2, f = __builtin_amdgcn_exp2f(dl);
      o = mfma32(v1, p1, o);
      p1 = (u32x4){0u, 0u, 0u, 0u};
#pragma unroll
      for (int r = 0; r < 16; ++r) { sA[r] += dl; cin[r] = -nb2; }
#pragma unroll
      for (int r = 0; r < 9; ++r) o[r] *= f;
      asm volatile("" : "+{v[160:175]}"(cin), "+{v[244:247]}"(p1), "+{v[176:191]}"(o), "+{v[192:207]}"(sA));
    }
  }
  __builtin_amdgcn_s_setprio(0);
  ATT_FILL(hbo + 112 * 1024, hbo + 168 * 1024, w == 7 ? WS_ST + rb * 8192 + m * 1024 : WS_END);
  CFENCE();
  unsigned tlb;
  asm volatile("global_load_dword %0, %1, %2" : "=&v"(tlb) : "v"(0u), "s"(out + (size_t)(64 * rb + 8 * w) * D_) : "memory");
  u32x4 af = LD16(l16, WS_AUXF + (bid & 15) * 1024);
  u32x4 xin = __builtin_amdgcn_raw_buffer_load_b128(xs, c * 1024 + g * 16, (64 * rb + 16 * (w & 3)) * 1024 + 128 * m + 64 * (w >> 2), 0);
  __builtin_amdgcn_s_setprio(3);
  ATT_PLAIN();
#pragma unroll 1
  for (int spins = 0; spins < SPIN_CAP && __any(umax4(af) == SENT); ++spins) {
    __builtin_amdgcn_s_sleep(8);
    RELOAD(af, l16, WS_AUXF + (bid & 15) * 1024);
  }
  if (bid == 0 && tid == 0) __hip_atomic_store((GAS u64*)(ws + WS_EPOCH), epoch, RLX_AGENT);
  asm volatile("" : "+{v[252:255]}"(xin) : "v"(tlb));
  u32x4 tb0, tb1;
  BL16C(tb0, l16, WS_MODP + w * 1024); BL16C(tb1, l16b, WS_MODP + w * 1024);
  u32x16 w1t[8];
  unsigned char* wpre = ws + WS_W1 + nt * 32768 + 4096;
  __builtin_amdgcn_s_setprio(3);
  SBAR();
  asm volatile(ATT_ASM(GL("v[32:35]", "%[va]", "-4096"),
                         GL("v[36:39]", "%[va]", "-3072"),
                         GL("v[40:43]", "%[va]", "-2048"),
                         GL("v[44:47]", "%[va]", "-1024"),
                         GL("v[48:51]", "%[va]", "0"),
                         GL("v[52:55]", "%[va]", "1024"),
                         GL("v[56:59]", "%[va]", "2048"),
                         GL("v[60:63]", "%[va]", "3072"),
                         GL("v[64:67]", "%[vb]", "-4096"),
                         GL("v[68:71]", "%[vb]", "-3072"),
                         GL("v[72:75]", "%[vb]", "-2048"),
                         GL("v[76:79]", "%[vb]", "-1024"),
                         GL("v[80:83]", "%[vb]", "0"),
                         GL("v[84:87]", "%[vb]", "1024"),
                         GL("v[88:91]", "%[vb]", "2048"),
                         GL("v[92:95]", "%[vb]", "3072"))
               : ATT_STATE, "=&{v[32:47]}"(w1t[0]), "=&{v[48:63]}"(w1t[1]), "=&{v[64:79]}"(w1t[2]), "=&{v[80:95]}"(w1t[3])
               : ATT_IN, [va] "v"(l16), [vb] "v"(l16b), [sb] "s"(wpre) : ATT_CLOB);
  SBAR();
  asm volatile("s_waitcnt vmcnt(16)" : "+v"(tb0), "+v"(tb1) :: "memory");
#pragma unroll 1
  for (int spins = 0; spins < SPIN_CAP && __any(max(umax4(tb0), umax4(tb1)) == SENT); ++spins) {
    __builtin_amdgcn_s_sleep(8);
    RELOAD(tb0, l16, WS_MODP + w * 1024); RELOAD(tb1, l16b, WS_MODP + w * 1024);
  }
  {
    const f32x4 t = __builtin_bit_cast(f32x4, tb0) + __builtin_bit_cast(f32x4, tb1);
    u32x2 tp = {pack2(t[0], t[1]), pack2(t[2], t[3])};
    if ((w >> 1) == 2) tp = (u32x2){pack2(1.f + bflo(tp[0]), 1.f + bfhi(tp[0])), pack2(1.f + bflo(tp[1]), 1.f + bfhi(tp[1]))};
    *(LAS u32x2*)(L + L_TAB + w * 512 + (l16 >> 1)) = tp;
    *(LAS unsigned*)(L + L_ZERO + w * 256 + (l16 >> 2)) = 0u;
  }
  lds_arrive(L + L_CNT + 24);
  __builtin_amdgcn_s_setprio(2);
  asm volatile(ATT_ASM(GL("v[96:99]", "%[va]", "-4096"),
                         GL("v[100:103]", "%[va]", "-3072"),
                         GL("v[104:107]", "%[va]", "-2048"),
                         GL("v[108:111]", "%[va]", "-1024"),
                         GL("v[112:115]", "%[va]", "0"),
                         GL("v[116:119]", "%[va]", "1024"),
                         GL("v[120:123]", "%[va]", "2048"),
                         GL("v[124:127]", "%[va]", "3072"),
                         GL("v[128:131]", "%[vb]", "-4096"),
                         GL("v[132:135]", "%[vb]", "-3072"),
                         GL("v[136:139]", "%[vb]", "-2048"),
                         GL("v[140:143]", "%[vb]", "-1024"),
                         GL("v[144:147]", "%[vb]", "0"),
                         GL("v[148:151]", "%[vb]", "1024"),
                         GL("v[152:155]", "%[vb]", "2048"),
                         GL("v[156:159]", "%[vb]", "3072"))
               : ATT_STATE, "=&{v[96:111]}"(w1t[4]), "=&{v[112:127]}"(w1t[5]), "=&{v[128:143]}"(w1t[6]), "=&{v[144:159]}"(w1t[7])
               : ATT_IN, [va] "v"(l16), [vb] "v"(l16b), [sb] "s"(wpre + 16384) : ATT_CLOB);
  SBAR();
  lds_wait<8>(L + L_CNT + 24);
  __builtin_amdgcn_s_setprio(1);
  int hoff[7];
  {
    const int h0 = WS_HB + (rb * 224 + 7 * w) * 1024;
    int t = h0 + (m % 7) * 1024;
#pragma unroll
    for (int n = 0; n < 7; ++n) { PINS(t); hoff[n] = t; t += 1024; t = t == h0 + 7168 ? h0 : t; }
  }
  u32x16 w2a; u32x8 w2b;
#define W2OFF(v, i) (WS_W2 + (m * 224 + 28 * (v) + (i)) * 1024)
#define G2(R, OFF) "global_load_dwordx4 " R ", %[va], %[s2] offset:" OFF " sc1\n"
#define D2(OFF) "global_load_lds_dwordx4 %[va], %[s3] offset:" OFF " sc1\n"
  SBAR();
  asm volatile("s_mov_b32 m0, %[ld]\n"
               ATT_ASM(DMA("%[va]", "0") G2("v[0:3]", "-2048"), DMA("%[va]", "1024"), DMA("%[va]", "2048") G2("v[4:7]", "-1024"), DMA("%[va]", "3072") "s_add_u32 m0, m0, 0x2000\n",
                       DMA("%[vb]", "0") G2("v[8:11]", "0"), DMA("%[vb]", "1024"), DMA("%[vb]", "2048") G2("v[12:15]", "1024"), DMA("%[vb]", "3072") "s_mov_b32 m0, %[l3]\n",
                       D2("0") G2("v[16:19]", "2048"), D2("1024"), D2("2048") G2("v[20:23]", "3072"), D2("3072"), "", "", "", "")
               : ATT_STATE, "=&{v[0:15]}"(w2a), "=&{v[16:23]}"(w2b)
               : ATT_IN, [va] "v"(l16), [vb] "v"(l16b), [sb] "s"(ws + WS_WO + m * 65536 + kvw), [ld] "s"(lds0 + kvw),
                 [s2] "s"(ws + W2OFF(w, 2)), [s3] "s"(ws + W2OFF(w, 17)), [l3] "s"(lds0 + L_W2 + w * 4096) : ATT_CLOB, "scc");
  SBAR();
  __builtin_amdgcn_s_setprio(0);
  asm volatile("s_waitcnt vmcnt(0)" : "+{v[32:47]}"(w1t[0]), "+{v[48:63]}"(w1t[1]), "+{v[64:79]}"(w1t[2]), "+{v[80:95]}"(w1t[3]),
               "+{v[96:111]}"(w1t[4]), "+{v[112:127]}"(w1t[5]), "+{v[128:143]}"(w1t[6]), "+{v[144:159]}"(w1t[7]), "+{v[0:15]}"(w2a), "+{v[16:23]}"(w2b) :: "memory");
  asm volatile("v_mfma_f32_32x32x16_bf16 v[176:191], v[236:239], v[244:247], v[176:191]\n\ts_nop 7\n\ts_nop 3" : "+{v[176:191]}"(o) : "{v[236:239]}"(v1), "{v[244:247]}"(p1));
  {
  const int lane = fresh_lane();
  const int g = lane >> 4, c = lane & 15, hi = lane >> 5, c32 = lane & 31;
  u32x4 of;
#define O_PACK() do { const float rl = __builtin_amdgcn_rcpf(o[8]); \
    _Pragma("unroll") for (int q = 0; q < 4; ++q) of[q] = min(pack2(o[2 * q] * rl, o[2 * q + 1] * rl), SENT - 1u); } while (0)
  O_PACK();
  if (__builtin_expect(!__all(o[8] >= 5.421010862427522e-20f), 0)) {
    f32x16 z;
#pragma unroll
    for (int r = 0; r < 16; ++r) { z[r] = 0.f; o[r] = 0.f; }
    float mrun = -INFINITY;
    const unsigned ov = (c32 >= 16 && (c & 11) == 0) ? 0x3f803f80u : 0u;
#pragma unroll 1
    for (int t = 0; t < 64; ++t) {
      const int tb = WS_KV + (t >> 3) * 16384 + (t & 7) * 1024;
      const u32x4 kk = LD16(lane * 16, tb);
      u32x4 va = LD16(hi * 256 + c * 16, tb + 8192);
      u32x4 vb = LD16(hi * 256 + c * 16, tb + 8192 + 512);
      if (c32 >= 16) { va = (u32x4){ov, ov, ov, ov}; vb = va; }
      const f32x16 s = mfma32(kk, qf, z);
      float tm = s[0];
#pragma unroll
      for (int r = 1; r < 16; ++r) tm = fmaxf(tm, s[r]);
      const float mn = fmaxf(mrun, max32(tm));
      const float al = __builtin_amdgcn_exp2f(mrun - mn);
      mrun = mn;
      u32x4 q0, q1;
#pragma unroll
      for (int q = 0; q < 4; ++q) {
        q0[q] = pack2(__builtin_amdgcn_exp2f(s[2 * q] - mn), __builtin_amdgcn_exp2f(s[2 * q + 1] - mn));
        q1[q] = pack2(__builtin_amdgcn_exp2f(s[8 + 2 * q] - mn), __builtin_amdgcn_exp2f(s[8 + 2 * q + 1] - mn));
      }
#pragma unroll
      for (int r = 0; r < 16; ++r) o[r] *= al;
      o = mfma32(va, q0, o);
      o = mfma32(vb, q1, o);
    }
    O_PACK();
  }
#define HOLD() asm volatile("" : "+{v[32:47]}"(w1t[0]), "+{v[48:63]}"(w1t[1]), "+{v[64:79]}"(w1t[2]), "+{v[80:95]}"(w1t[3]), "+{v[96:111]}"(w1t[4]), \
    "+{v[112:127]}"(w1t[5]), "+{v[128:143]}"(w1t[6]), "+{v[144:159]}"(w1t[7]), "+{v[0:15]}"(w2a), "+{v[16:23]}"(w2b))
  HOLD();
  asm volatile("" : "+{v[252:255]}"(xin));
#define CANON(v) do { _Pragma("unroll") for (int q_ = 0; q_ < 4; ++q_) v[q_] = min(v[q_], SENT - 1u); } while (0)
  ST16(of, lane * 16, WS_OB + ((rb * 2 + rt) * 32 + 4 * m + hh) * 1024);
  f32x4 uv[2] = {{0.f, 0.f, 0.f, 0.f}, {0.f, 0.f, 0.f, 0.f}};
  const LAS unsigned char* tp = L + (c < 2 ? L_TAB + 2048 - c * 1024 + g * 16 : L_ZERO);
#define UV_PART(K0, K1) do { u32x4 tf_[K1 - K0]; SBAR(); \
    _Pragma("unroll") for (int ks = K0; ks < K1; ++ks) tf_[ks - K0] = *(const LAS u32x4*)(tp + ks * 64); \
    SBAR(); \
    _Pragma("unroll") for (int ks = K0; ks < K1; ++ks) { uv[0] = mfma16((ks & 1) ? SUB4(w1t[ks >> 1], 2) : SUB4(w1t[ks >> 1], 0), tf_[ks - K0], uv[0]); uv[1] = mfma16((ks & 1) ? SUB4(w1t[ks >> 1], 3) : SUB4(w1t[ks >> 1], 1), tf_[ks - K0], uv[1]); } \
    PIN(uv[0]); PIN(uv[1]); SBAR(); } while (0)
  UV_PART(0, UVS);
  const int rs4 = w & 3, kh = w >> 2;
  const int ecol = 64 * m + 32 * kh + 8 * g;
  u32x4 x1p;
  {
    u32x4 bf[8];
    int oo[4];
#pragma unroll
    for (int i = 0; i < 4; ++i) { oo[i] = WS_OB + ((rb * 2 + (rs4 >> 1)) * 32 + 16 * kh + 4 * i) * 1024 + (rs4 & 1) * 256; PINS(oo[i]); }
    unsigned va = g * 512 + c * 16, wpo = kh * 32768 + lane * 16, exw = 98304 + w * 2048 + lane * 16, tbo = L_TAB + ecol * 2;
    unsigned svo = c * 8 + hi * 4 + ((g & 1) << 30);
    int sso = WS_ST + rb * 8192 + (2 * m + kh) * 512 + rs4 * 128;
    PIN(va); PIN(wpo); PIN(exw); PIN(tbo); PIN(svo); PINS(sso);
    f32x4 acc[4];
#pragma unroll
    for (int t = 0; t < 4; ++t) acc[t] = (f32x4){0.f, 0.f, 0.f, 0.f};
#define WO_RD(n) (*(const LAS u32x4*)(L + wpo + (n) * 1024))
    LDS_BAR();
    asm volatile("buffer_load_dwordx4 v[160:163], %[vo], %[rs], %[so] offen offset:0 sc1\nbuffer_load_dwordx4 v[164:167], %[vo], %[rs], %[so] offen offset:2048 sc1\n"
        : "=&{v[160:163]}"(bf[0]), "=&{v[164:167]}"(bf[1]) : [vo] "v"(va), [rs] "s"(rs), [so] "s"(oo[0]) : "memory");
    asm volatile("buffer_load_dwordx4 v[168:171], %[vo], %[rs], %[so] offen offset:0 sc1\nbuffer_load_dwordx4 v[172:175], %[vo], %[rs], %[so] offen offset:2048 sc1\n"
        : "=&{v[168:171]}"(bf[2]), "=&{v[172:175]}"(bf[3]) : [vo] "v"(va), [rs] "s"(rs), [so] "s"(oo[1]) : "memory");
    asm volatile("buffer_load_dwordx4 v[176:179], %[vo], %[rs], %[so] offen offset:0 sc1\nbuffer_load_dwordx4 v[180:183], %[vo], %[rs], %[so] offen offset:2048 sc1\n"
        : "=&{v[176:179]}"(bf[4]), "=&{v[180:183]}"(bf[5]) : [vo] "v"(va), [rs] "s"(rs), [so] "s"(oo[2]) : "memory");
    asm volatile("buffer_load_dwordx4 v[184:187], %[vo], %[rs], %[so] offen offset:0 sc1\nbuffer_load_dwordx4 v[188:191], %[vo], %[rs], %[so] offen offset:2048 sc1\n"
        : "=&{v[184:187]}"(bf[6]), "=&{v[188:191]}"(bf[7]) : [vo] "v"(va), [rs] "s"(rs), [so] "s"(oo[3]) : "memory");
    u32x4 wq4[32];
#pragma unroll
    for (int n = 0; n < WRD; ++n) wq4[n] = WO_RD(n);
    SBAR();
#define WO_STEP(i) _Pragma("unroll") for (int n = 4 * (i); n < 4 * (i) + 4; ++n) { \
      if (n + WRD < 32) wq4[n + WRD] = WO_RD(n + WRD); \
      acc[n & 3] = mfma16(wq4[n], bf[i], acc[n & 3]); SBAR(); }
    { unsigned t_, n_; asm volatile("s_waitcnt vmcnt(6)\ns_mov_b32 %[n], 0x20000\n1:\n"
        "v_max3_u32 %[t], v160, v161, v162\nv_max3_u32 %[t], %[t], v163, v164\nv_max3_u32 %[t], %[t], v165, v166\nv_max_u32 %[t], %[t], v167\n"
        "v_cmp_eq_u32 vcc, -1, %[t]\ns_cbranch_vccz 2f\ns_sleep 1\n"
        "buffer_load_dwordx4 v[160:163], %[vo], %[rs], %[so] offen offset:0 sc1\nbuffer_load_dwordx4 v[164:167], %[vo], %[rs], %[so] offen offset:2048 sc1\n"
        "s_waitcnt vmcnt(0)\ns_sub_u32 %[n], %[n], 1\ns_cbranch_scc0 1b\n2:\n"
        : "+{v[160:163]}"(bf[0]), "+{v[164:167]}"(bf[1]), [t] "=&v"(t_), [n] "=&s"(n_) : [vo] "v"(va), [rs] "s"(rs), [so] "s"(oo[0]) : "memory", "vcc", "scc"); }
    WO_STEP(0) WO_STEP(1)
    { unsigned t_, n_; asm volatile("s_waitcnt vmcnt(4)\ns_mov_b32 %[n], 0x20000\n1:\n"
        "v_max3_u32 %[t], v168, v169, v170\nv_max3_u32 %[t], %[t], v171, v172\nv_max3_u32 %[t], %[t], v173, v174\nv_max_u32 %[t], %[t], v175\n"
        "v_cmp_eq_u32 vcc, -1, %[t]\ns_cbranch_vccz 2f\ns_sleep 1\n"
        "buffer_load_dwordx4 v[168:171], %[vo], %[rs], %[so] offen offset:0 sc1\nbuffer_load_dwordx4 v[172:175], %[vo], %[rs], %[so] offen offset:2048 sc1\n"
        "s_waitcnt vmcnt(0)\ns_sub_u32 %[n], %[n], 1\ns_cbranch_scc0 1b\n2:\n"
        : "+{v[168:171]}"(bf[2]), "+{v[172:175]}"(bf[3]), [t] "=&v"(t_), [n] "=&s"(n_) : [vo] "v"(va), [rs] "s"(rs), [so] "s"(oo[1]) : "memory", "vcc", "scc"); }
    WO_STEP(2) WO_STEP(3)
    { unsigned t_, n_; asm volatile("s_waitcnt vmcnt(2)\ns_mov_b32 %[n], 0x20000\n1:\n"
        "v_max3_u32 %[t], v176, v177, v178\nv_max3_u32 %[t], %[t], v179, v180\nv_max3_u32 %[t], %[t], v181, v182\nv_max_u32 %[t], %[t], v183\n"
        "v_cmp_eq_u32 vcc, -1, %[t]\ns_cbranch_vccz 2f\ns_sleep 1\n"
        "buffer_load_dwordx4 v[176:179], %[vo], %[rs], %[so] offen offset:0 sc1\nbuffer_load_dwordx4 v[180:183], %[vo], %[rs], %[so] offen offset:2048 sc1\n"
        "s_waitcnt vmcnt(0)\ns_sub_u32 %[n], %[n], 1\ns_cbranch_scc0 1b\n2:\n"
        : "+{v[176:179]}"(bf[4]), "+{v[180:183]}"(bf[5]), [t] "=&v"(t_), [n] "=&s"(n_) : [vo] "v"(va), [rs] "s"(rs), [so] "s"(oo[2]) : "memory", "vcc", "scc"); }
    WO_STEP(4) WO_STEP(5)
    { unsigned t_, n_; asm volatile("s_waitcnt vmcnt(0)\ns_mov_b32 %[n], 0x20000\n1:\n"
        "v_max3_u32 %[t], v184, v185, v186\nv_max3_u32 %[t], %[t], v187, v188\nv_max3_u32 %[t], %[t], v189, v190\nv_max_u32 %[t], %[t], v191\n"
        "v_cmp_eq_u32 vcc, -1, %[t]\ns_cbranch_vccz 2f\ns_sleep 1\n"
        "buffer_load_dwordx4 v[184:187], %[vo], %[rs], %[so] offen offset:0 sc1\nbuffer_load_dwordx4 v[188:191], %[vo], %[rs], %[so] offen offset:2048 sc1\n"
        "s_waitcnt vmcnt(0)\ns_sub_u32 %[n], %[n], 1\ns_cbranch_scc0 1b\n2:\n"
        : "+{v[184:187]}"(bf[6]), "+{v[188:191]}"(bf[7]), [t] "=&v"(t_), [n] "=&s"(n_) : [vo] "v"(va), [rs] "s"(rs), [so] "s"(oo[3]) : "memory", "vcc", "scc"); }
    const u32x4 ga = *(const LAS u32x4*)(L + tbo), cb = *(const LAS u32x4*)(L + tbo + 2048);
    WO_STEP(6) WO_STEP(7)
    HOLD();
    asm volatile("" : "+{v[252:255]}"(xin));
    *(LAS f32x4*)(L + exw) = acc[2];
    *(LAS f32x4*)(L + exw + 1024) = acc[3];
    f32x2 gf[4], xf[4], cf[4];
#pragma unroll
    for (int q = 0; q < 4; ++q) { gf[q] = (f32x2){bflo(ga[q]), bfhi(ga[q])}; xf[q] = (f32x2){bflo(xin[q]), bfhi(xin[q])}; cf[q] = (f32x2){bflo(cb[q]), bfhi(cb[q])}; }
#pragma unroll
    for (int q = 0; q < 4; ++q) xf[q] = __builtin_elementwise_fma(gf[q], (f32x2){acc[q >> 1][2 * (q & 1)], acc[q >> 1][2 * (q & 1) + 1]}, xf[q]);
    asm volatile("" : "+v"(gf[0]), "+v"(gf[1]), "+v"(gf[2]), "+v"(gf[3]), "+v"(xf[0]), "+v"(xf[1]), "+v"(xf[2]), "+v"(xf[3]), "+v"(cf[0]), "+v"(cf[1]), "+v"(cf[2]), "+v"(cf[3]));
    SBAR();
    __syncthreads();
    const f32x4 r0 = *(const LAS f32x4*)(L + (exw ^ 8192)), r1 = *(const LAS f32x4*)(L + (exw ^ 8192) + 1024);
    u32x4 y;
#pragma unroll
    for (int q = 0; q < 4; ++q) {
      const f32x2 t = __builtin_elementwise_fma(gf[q], q < 2 ? (f32x2){r0[2 * (q & 1)], r0[2 * (q & 1) + 1]} : (f32x2){r1[2 * (q & 1)], r1[2 * (q & 1) + 1]}, xf[q]);
      x1p[q] = pack2(t[0], t[1]);
      const f32x2 u = (f32x2){bflo(x1p[q]), bfhi(x1p[q])} * cf[q];
      y[q] = pack2(u[0], u[1]);
    }
    CANON(y);
    ST16(y, lane * 16, WS_YB + rb * 65536 + m * 8192 + w * 1024);
    float st = fold32(dot2bf(x1p[0], 0x3f803f80u, dot2bf(x1p[1], 0x3f803f80u, dot2bf(x1p[2], 0x3f803f80u, dot2bf(x1p[3], 0x3f803f80u, 0.f)))), knorm2(x1p));
    st = fold16(st, st);
    __builtin_amdgcn_raw_buffer_store_b32(min(__float_as_uint(st), SENT - 1u), rs, svo, sso, AUX_SC1);
  }
  asm volatile("" : "+{v[248:251]}"(x1p));
  UV_PART(UVS, 16);
  f32x16 uuvv;
#pragma unroll
  for (int j = 0; j < 8; ++j) { uuvv[j] = dpp<0x150>(uv[j >> 2][j & 3]); uuvv[8 + j] = dpp<0x151>(uv[j >> 2][j & 3]) * -LOG2E; }
  asm volatile("" : "+{v[208:223]}"(uuvv));

  {
    u32x4 yr[8], sp;
    int yo[2], so = WS_ST + rb * 8192 + w * 64;
#pragma unroll
    for (int i = 0; i < 2; ++i) { yo[i] = WS_YB + rb * 65536 + (w * 8 + 4 * i) * 1024; PINS(yo[i]); }
    unsigned va = lane * 16, vs = (lane >> 2) * 512 + (lane & 3) * 16, yl = w * 8192 + lane * 16;
    PINS(so); PIN(va); PIN(vs); PIN(yl);
    LDS_BAR();
    asm volatile("buffer_load_dwordx4 v[160:163], %[vo], %[rs], %[so] offen offset:0 sc1\nbuffer_load_dwordx4 v[164:167], %[vo], %[rs], %[so] offen offset:1024 sc1\nbuffer_load_dwordx4 v[168:171], %[vo], %[rs], %[so] offen offset:2048 sc1\nbuffer_load_dwordx4 v[172:175], %[vo], %[rs], %[so] offen offset:3072 sc1\n"
        : "=&{v[160:163]}"(yr[0]), "=&{v[164:167]}"(yr[1]), "=&{v[168:171]}"(yr[2]), "=&{v[172:175]}"(yr[3]) : [vo] "v"(va), [rs] "s"(rs), [so] "s"(yo[0]) : "memory");
    asm volatile("buffer_load_dwordx4 v[176:179], %[vo], %[rs], %[so] offen offset:0 sc1\nbuffer_load_dwordx4 v[180:183], %[vo], %[rs], %[so] offen offset:1024 sc1\nbuffer_load_dwordx4 v[184:187], %[vo], %[rs], %[so] offen offset:2048 sc1\nbuffer_load_dwordx4 v[188:191], %[vo], %[rs], %[so] offen offset:3072 sc1\n"
        : "=&{v[176:179]}"(yr[4]), "=&{v[180:183]}"(yr[5]), "=&{v[184:187]}"(yr[6]), "=&{v[188:191]}"(yr[7]) : [vo] "v"(va), [rs] "s"(rs), [so] "s"(yo[1]) : "memory");
    asm volatile("buffer_load_dwordx4 v[192:195], %[vo], %[rs], %[so] offen offset:0 sc1\n"
        : "=&{v[192:195]}"(sp) : [vo] "v"(vs), [rs] "s"(rs), [so] "s"(so) : "memory");
    { unsigned t_, n_; asm volatile("s_waitcnt vmcnt(5)\ns_mov_b32 %[n], 0x20000\n1:\n"
        "v_max3_u32 %[t], v160, v161, v162\nv_max3_u32 %[t], %[t], v163, v164\nv_max3_u32 %[t], %[t], v165, v166\nv_max3_u32 %[t], %[t], v167, v168\nv_max3_u32 %[t], %[t], v169, v170\nv_max3_u32 %[t], %[t], v171, v172\nv_max3_u32 %[t], %[t], v173, v174\nv_max_u32 %[t], %[t], v175\n"
        "v_cmp_eq_u32 vcc, -1, %[t]\ns_cbranch_vccz 2f\ns_sleep 1\n"
        "buffer_load_dwordx4 v[160:163], %[vo], %[rs], %[so] offen offset:0 sc1\nbuffer_load_dwordx4 v[164:167], %[vo], %[rs], %[so] offen offset:1024 sc1\nbuffer_load_dwordx4 v[168:171], %[vo], %[rs], %[so] offen offset:2048 sc1\nbuffer_load_dwordx4 v[172:175], %[vo], %[rs], %[so] offen offset:3072 sc1\n"
        "s_waitcnt vmcnt(0)\ns_sub_u32 %[n], %[n], 1\ns_cbranch_scc0 1b\n2:\n"
        : "+{v[160:163]}"(yr[0]), "+{v[164:167]}"(yr[1]), "+{v[168:171]}"(yr[2]), "+{v[172:175]}"(yr[3]), [t] "=&v"(t_), [n] "=&s"(n_) : [vo] "v"(va), [rs] "s"(rs), [so] "s"(yo[0]) : "memory", "vcc", "scc"); }
    *(LAS u32x4*)(L + yl + 0) = yr[0]; *(LAS u32x4*)(L + yl + 1024) = yr[1]; *(LAS u32x4*)(L + yl + 2048) = yr[2]; *(LAS u32x4*)(L + yl + 3072) = yr[3];
    { unsigned t_, n_; asm volatile("s_waitcnt vmcnt(1)\ns_mov_b32 %[n], 0x20000\n1:\n"
        "v_max3_u32 %[t], v176, v177, v178\nv_max3_u32 %[t], %[t], v179, v180\nv_max3_u32 %[t], %[t], v181, v182\nv_max3_u32 %[t], %[t], v183, v184\nv_max3_u32 %[t], %[t], v185, v186\nv_max3_u32 %[t], %[t], v187, v188\nv_max3_u32 %[t], %[t], v189, v190\nv_max_u32 %[t], %[t], v191\n"
        "v_cmp_eq_u32 vcc, -1, %[t]\ns_cbranch_vccz 2f\ns_sleep 1\n"
        "buffer_load_dwordx4 v[176:179], %[vo], %[rs], %[so] offen offset:0 sc1\nbuffer_load_dwordx4 v[180:183], %[vo], %[rs], %[so] offen offset:1024 sc1\nbuffer_load_dwordx4 v[184:187], %[vo], %[rs], %[so] offen offset:2048 sc1\nbuffer_load_dwordx4 v[188:191], %[vo], %[rs], %[so] offen offset:3072 sc1\n"
        "s_waitcnt vmcnt(0)\ns_sub_u32 %[n], %[n], 1\ns_cbranch_scc0 1b\n2:\n"
        : "+{v[176:179]}"(yr[4]), "+{v[180:183]}"(yr[5]), "+{v[184:187]}"(yr[6]), "+{v[188:191]}"(yr[7]), [t] "=&v"(t_), [n] "=&s"(n_) : [vo] "v"(va), [rs] "s"(rs), [so] "s"(yo[1]) : "memory", "vcc", "scc"); }
    *(LAS u32x4*)(L + yl + 4096) = yr[4]; *(LAS u32x4*)(L + yl + 5120) = yr[5]; *(LAS u32x4*)(L + yl + 6144) = yr[6]; *(LAS u32x4*)(L + yl + 7168) = yr[7];
    { unsigned t_, n_; asm volatile("s_waitcnt vmcnt(0)\ns_mov_b32 %[n], 0x20000\n1:\n"
        "v_max3_u32 %[t], v192, v193, v194\nv_max_u32 %[t], %[t], v195\n"
        "v_cmp_eq_u32 vcc, -1, %[t]\ns_cbranch_vccz 2f\ns_sleep 1\n"
        "buffer_load_dwordx4 v[192:195], %[vo], %[rs], %[so] offen offset:0 sc1\n"
        "s_waitcnt vmcnt(0)\ns_sub_u32 %[n], %[n], 1\ns_cbranch_scc0 1b\n2:\n"
        : "+{v[192:195]}"(sp), [t] "=&v"(t_), [n] "=&s"(n_) : [vo] "v"(vs), [rs] "s"(rs), [so] "s"(so) : "memory", "vcc", "scc"); }
    float s1 = fold32(__uint_as_float(sp[0]), __uint_as_float(sp[2])), s2 = fold32(__uint_as_float(sp[1]), __uint_as_float(sp[3]));
    s1 = fold16(s1, s1); s2 = fold16(s2, s2);
    s1 += dpp<0x128>(s1); s2 += dpp<0x128>(s2);
    s1 += dpp<0x124>(s1); s2 += dpp<0x124>(s2);
    const float mu = s1 * (1.f / D_);
    const float rstd = __builtin_amdgcn_rsqf(fmaxf(fmaf(-mu, mu, s2 * (1.f / D_)), 0.f) + LN_EPS);
    if (c32 < 4) *(LAS f32x2*)(L + L_STAT + (8 * w + 2 * c32 + hi) * 8) = (f32x2){rstd * -LOG2E, -mu};
  }
  {
    int w2o = W2OFF(w, 6), hb = hbo, ps = W2OFF(0, 21);
    unsigned n_, t_, rp_, l16 = lane * 16;
    PINS(w2o); PINS(hb); PINS(ps);
    asm volatile("" : "+{v252}"(l16));
    SBAR();
    __syncthreads();
    SBAR();
    asm volatile(FFN_ASM
                 : "+{v[208:223]}"(uuvv), "+{v[0:15]}"(w2a), "+{v[16:23]}"(w2b), "+{v[32:47]}"(w1t[0]), "+{v[48:63]}"(w1t[1]), "+{v[64:79]}"(w1t[2]), "+{v[80:95]}"(w1t[3]),
                   "+{v[96:111]}"(w1t[4]), "+{v[112:127]}"(w1t[5]), "+{v[128:143]}"(w1t[6]), "+{v[144:159]}"(w1t[7]),
                   [w2] "+s"(w2o), [hb] "+s"(hb), [ps] "+s"(ps), [n] "=&s"(n_), [t] "=&s"(t_), [rp] "=&s"(rp_)
                 : "{v252}"(l16), [w] "s"(w), [rs] "s"(rs), [h0] "s"(hoff[0]), [h1] "s"(hoff[1]), [h2] "s"(hoff[2]), [h3] "s"(hoff[3]),
                   [h4] "s"(hoff[4]), [h5] "s"(hoff[5]), [h6] "s"(hoff[6]), [lc] "i"(L_CNT), [ls] "i"(L_STAT), [pk] "i"(L_W2 + 32768),
                   [rdo] "s"(w * 16384), [pao] "s"(L_W2 + w * 4096), [pbo] "s"(L_W2 + 32768 + w * 7168)
                 : "v24", "v25", "v26", "v27", "v28", "v29", "v30", "v31", "v160", "v161", "v162", "v163", "v164", "v165", "v166", "v167", "v168", "v169", "v170", "v171", "v172", "v173", "v174", "v175", "v176", "v177", "v178", "v179", "v180", "v181", "v182", "v183", "v184", "v185", "v186", "v187", "v188", "v189", "v190", "v191", "v192", "v193", "v194", "v195", "v196", "v197", "v198", "v199", "v200", "v201", "v202", "v203", "v204", "v205", "v206", "v207", "v224", "v225", "v226", "v227", "v228", "v229", "v230", "v231", "v232", "v233", "v234", "v235", "v236", "v237", "v238", "v239", "v240", "v241", "v242", "v243", "v244", "v245", "v246", "v247", "v253", "v254", "v255", "memory", "vcc", "scc");
    const int lane = fresh_lane(), g = lane >> 4, c = lane & 15;
    const int erow = 64 * rb + 16 * rs4 + c, ecol = 64 * m + 32 * kh + 8 * g;
    const u32x4 gb = *(const LAS u32x4*)(L + L_TAB + 3072 + ecol * 2);
    f32x2 gf[4], xf[4];
#pragma unroll
    for (int q = 0; q < 4; ++q) { gf[q] = (f32x2){bflo(gb[q]), bfhi(gb[q])} * -0.6931471805599453f; xf[q] = (f32x2){bflo(x1p[q]), bfhi(x1p[q])}; }
    unsigned fp = (rs4 * 4 + 2 * kh) * 1024 + lane * 16, fp2 = fp + 65536, oo = (erow * D_ + ecol) * 2;
    asm volatile("" : "+v"(gf[0]), "+v"(gf[1]), "+v"(gf[2]), "+v"(gf[3]), "+v"(xf[0]), "+v"(xf[1]), "+v"(xf[2]), "+v"(xf[3]), "+v"(fp), "+v"(fp2), "+v"(oo));
    const __amdgpu_buffer_rsrc_t os = __builtin_amdgcn_make_buffer_rsrc(out, 0, 2048 * 512 * 2, 0x00020000);
    SBAR();
    asm volatile("s_waitcnt lgkmcnt(0)\n\ts_barrier" ::: "memory");
    f32x4 pa[8], pb[8];
#pragma unroll
    for (int p = 0; p < 8; ++p) { pa[p] = *(const LAS f32x4*)(L + (p < 4 ? fp : fp2) + (p & 3) * 16384); pb[p] = *(const LAS f32x4*)(L + (p < 4 ? fp : fp2) + (p & 3) * 16384 + 1024); }
    SBAR();
    const f32x4 a0 = ((pa[0] + pa[1]) + (pa[2] + pa[3])) + ((pa[4] + pa[5]) + (pa[6] + pa[7]));
    const f32x4 a1 = ((pb[0] + pb[1]) + (pb[2] + pb[3])) + ((pb[4] + pb[5]) + (pb[6] + pb[7]));
    u32x4 xo;
#pragma unroll
    for (int q = 0; q < 4; ++q) {
      const f32x2 t = __builtin_elementwise_fma(gf[q], q < 2 ? (f32x2){a0[2 * (q & 1)], a0[2 * (q & 1) + 1]} : (f32x2){a1[2 * (q & 1)], a1[2 * (q & 1) + 1]}, xf[q]);
      xo[q] = pack2(t[0], t[1]);
    }
    __builtin_amdgcn_raw_buffer_store_b128(xo, os, oo, 0, AUX_SC1);
  }
  asm volatile(".Lfb_code_hi:");
  }
}

extern "C" void kernel_launch(void* const* d_in, const int* in_sizes, int n_in, void* d_out, int out_size, void* d_ws, size_t ws_size,
                              hipStream_t stream) {
  fused_block<<<256, 512, 0, stream>>>((const u16*)d_in[0], (const u16*)d_in[1], (const u16*)d_in[2], (const u16*)d_in[3],
                                       (const u16*)d_in[4], (const u16*)d_in[6], (const u16*)d_in[7], (const u16*)d_in[8],
                                       (const u16*)d_in[9], (unsigned char*)d_ws, (u16*)d_out);
}
```
